```python
import torch

def _softmax(x, axis=-1):
    return torch.softmax(x, dim=axis)
def _gelu(x):
    return torch.nn.functional.gelu(x, approximate="tanh")
def _silu(x):
    return torch.nn.functional.silu(x)
def _sigmoid(x):
    return torch.sigmoid(x)
def _layernorm(x, axis=-1, eps=1e-5):
    m = x.mean(dim=axis, keepdim=True)
    v = x.var(dim=axis, keepdim=True, unbiased=False)
    return (x - m) / torch.sqrt(v + eps)
def _scatter_add(tgt, idx, src, axis=0):
    return tgt.clone().index_add_(axis, idx, src)
def _segment_sum(src, seg, n):
    out = torch.zeros((n,) + src.shape[1:], dtype=src.dtype)
    return out.index_add_(0, seg, src)
def _pad(x, pw):
    flat = tuple(v for pair in reversed(pw) for v in pair)
    return torch.nn.functional.pad(x, flat)

kernel_name = 'interleave_layers__ssm_conv_h3__ssm_conv_s4d__prompt'

def setup_inputs():

    _w = (lambda a: (lambda t: t.to(torch.bfloat16) if t.is_floating_point() else t)(torch.as_tensor(a)))
    def _A_setup():
        B, S, D = 1, 2048, 512
        ks = 4
        return {
            "q": _w((lambda *s: torch.randn(*s, dtype=torch.bfloat16) * 0.05)(B, S, D)),
            "k": _w((lambda *s: torch.randn(*s, dtype=torch.bfloat16) * 0.4)(B, S, D)),
            "v": _w((lambda *s: torch.randn(*s, dtype=torch.bfloat16) * 0.4)(B, S, D)),
            "shift_k": _w((lambda *s: torch.randn(*s, dtype=torch.bfloat16) * 0.4)(D, ks) * 0.5),
            "ssm_k": _w((lambda *s: torch.randn(*s, dtype=torch.bfloat16) * 0.4)(D, S) * (1.0 / S ** 0.5)),
            "ssm_D": _w((lambda *s: torch.randn(*s, dtype=torch.bfloat16) * 0.4)(D)),
        }
    _A_in = _A_setup()

    def _B_setup():
        Bsz, L, D, N = 1, 2048, 512, 128
        return {
            "u": _w((lambda *s: torch.randn(*s, dtype=torch.bfloat16) * 0.05)(Bsz, L, D)),
            "log_A_real": _w((lambda *s: torch.randn(*s, dtype=torch.bfloat16) * 6.4)(D, N) * 0.5),
            "A_imag": _w((lambda *s: torch.randn(*s, dtype=torch.bfloat16) * 0.05)(D, N) * 3.14),
            "C_real": _w((lambda *s: torch.randn(*s, dtype=torch.bfloat16) * 0.1)(D, N) / N ** 0.5),
            "C_imag": _w((lambda *s: torch.randn(*s, dtype=torch.bfloat16) * 0.1)(D, N) / N ** 0.5),
            "log_dt": _w((lambda *s: torch.randn(*s, dtype=torch.bfloat16) * 0.05)(D) * 0.5 - 4.0),
            "D_skip": _w((lambda *s: torch.randn(*s, dtype=torch.bfloat16) * 0.05)(D)),
        }
    _B_in = _B_setup()

    return {
        "A_q": _A_in["q"],
        "A_k": _A_in["k"],
        "A_v": _A_in["v"],
        "A_shift_k": _A_in["shift_k"],
        "A_ssm_k": _A_in["ssm_k"],
        "A_ssm_D": _A_in["ssm_D"],
        "B_u": _B_in["u"],
        "B_log_A_real": _B_in["log_A_real"],
        "B_A_imag": _B_in["A_imag"],
        "B_C_real": _B_in["C_real"],
        "B_C_imag": _B_in["C_imag"],
        "B_log_dt": _B_in["log_dt"],
        "B_D_skip": _B_in["D_skip"],
    }

def reference(A_q, A_k, A_v, A_shift_k, A_ssm_k, A_ssm_D, B_u, B_log_A_real, B_A_imag, B_C_real, B_C_imag, B_log_dt, B_D_skip):
    def _A_ref(q, k, v, shift_k, ssm_k, ssm_D):
        B, S, D = q.shape
        ks = shift_k.shape[-1]
        k_pad = _pad(k, ((0, 0), (ks - 1, 0), (0, 0)))
        k_shift = torch.zeros_like(k)
        for t in range(ks):
            k_shift = k_shift + k_pad[:, t:t + S, :] * shift_k[:, ks - 1 - t][None, None, :]
        kv = k_shift * v
        L = 2 * S
        kv_pad = _pad(kv, ((0, 0), (0, S), (0, 0)))
        kf_pad = _pad(ssm_k, ((0, 0), (0, S)))
        kv_f = (lambda x, axes=(-2,-1): torch.fft.rfft2(x.float(), dim=axes))(kv_pad[:, None, :, :], axes=(1, 2))
        kf_f = (lambda x, axes=(-2,-1): torch.fft.rfft2(x.float(), dim=axes))(kf_pad[:, None, :], axes=(1, 2))
        kf_f = (lambda x, *d: x.permute(*d))(kf_f, 1, 2, 0)
        y_f = kv_f * kf_f[None, :, :, :]
        y = (lambda x, s, axes=(-2,-1): torch.fft.irfft2(x, s=s, dim=axes).to(torch.bfloat16))(y_f, (1, L), axes=(1, 2))[:, 0, :S, :]
        y = y + kv * ssm_D[None, None, :]
        return q * y

    def _B_ref(u, log_A_real, A_imag, C_real, C_imag, log_dt, D_skip):
        L = u.shape[1]
        dt = torch.exp(log_dt)
        A = -torch.exp(log_A_real) + 1j * A_imag
        C = (C_real + 1j * C_imag) * dt[:, None]
        pos = (lambda x, dt: x.to(dt))(torch.arange(L), log_dt.dtype)
        P = torch.exp((A * dt[:, None])[:, :, None] * pos[None, None, :])
        K = 2.0 * torch.einsum("dn,dnl->dl", C, P).real
        L2 = 2 * L
        u_t = (lambda x, *d: x.permute(*d))(u, 0, 2, 1)
        Kf = (lambda x, *a, **k: torch.fft.rfft(x.float(), *a, **k))(K, L2)
        Uf = (lambda x, *a, **k: torch.fft.rfft(x.float(), *a, **k))(u_t, L2)
        y = (lambda x, *a, **k: torch.fft.irfft(x, *a, **k).to(torch.bfloat16))(Uf * Kf[None, :, :], L2)[:, :, :L]
        y = (lambda x, *d: x.permute(*d))(y, 0, 2, 1)
        return y + u * D_skip[None, None, :]

    x = A_q
    x = x + _A_ref(_layernorm(x, axis=-1), A_k, A_v, A_shift_k, A_ssm_k, A_ssm_D)
    x = x + _B_ref(_layernorm(x, axis=-1), B_log_A_real, B_A_imag, B_C_real, B_C_imag, B_log_dt, B_D_skip)
    x = x + _A_ref(_layernorm(x, axis=-1), A_k, A_v, A_shift_k, A_ssm_k, A_ssm_D)
    x = x + _B_ref(_layernorm(x, axis=-1), B_log_A_real, B_A_imag, B_C_real, B_C_imag, B_log_dt, B_D_skip)
    return x
```

```cpp
#include <hip/hip_runtime.h>
#include <hip/hip_bf16.h>
#include <stdint.h>

typedef unsigned short u16;
#define S_LEN 2048
#define D_DIM 512
#define N_ST 128

__device__ __forceinline__ float bf2f(u16 x) { return __uint_as_float(((unsigned)x) << 16); }
__device__ __forceinline__ u16 f2bf(float f) {
    unsigned u = __float_as_uint(f);
    if ((u & 0x7fffffffu) > 0x7f800000u) return (u16)((u >> 16) | 0x40);
    return (u16)((u + 0x7fffu + ((u >> 16) & 1u)) >> 16);
}
__device__ __forceinline__ float rbf(float f) { return bf2f(f2bf(f)); }

__global__ void k_kv(const u16* k, const u16* v, const u16* sk, float* kvT) {
    int idx = blockIdx.x * blockDim.x + threadIdx.x;
    int s = idx / D_DIM, d = idx % D_DIM;
    float acc = 0.f;
    for (int t = 0; t < 4; ++t) {
        int ss = s + t - 3;
        float kk = ss >= 0 ? bf2f(k[ss * D_DIM + d]) : 0.f;
        acc = rbf(acc + rbf(kk * bf2f(sk[d * 4 + 3 - t])));
    }
    kvT[d * S_LEN + s] = rbf(acc * bf2f(v[idx]));
}

__global__ void k_gen(const u16* logA, const u16* Aim, const u16* Cr, const u16* Ci, const u16* logdt, float* K) {
    int idx = blockIdx.x * blockDim.x + threadIdx.x;
    int d = idx / S_LEN, l = idx % S_LEN;
    float dt = rbf(expf(bf2f(logdt[d])));
    float pos = rbf((float)l);
    float acc = 0.f;
    for (int n = 0; n < N_ST; ++n) {
        float are = -rbf(expf(bf2f(logA[d * N_ST + n])));
        float aim = bf2f(Aim[d * N_ST + n]);
        float zr = are * dt, zi = aim * dt;
        float cr = bf2f(Cr[d * N_ST + n]) * dt, ci = bf2f(Ci[d * N_ST + n]) * dt;
        float wr = zr * pos, wi = zi * pos;
        float e = expf(wr);
        float sn, cs;
        sincosf(wi, &sn, &cs);
        acc += cr * (e * cs) - ci * (e * sn);
    }
    K[idx] = 2.f * acc;
}

__global__ void k_cvt(const u16* a, float* o) {
    int idx = blockIdx.x * blockDim.x + threadIdx.x;
    o[idx] = bf2f(a[idx]);
}

__global__ void k_conv(const float* K, const float* uT, float* yT) {
    __shared__ float sK[S_LEN], sU[S_LEN];
    int d = blockIdx.x;
    for (int i = threadIdx.x; i < S_LEN; i += blockDim.x) { sK[i] = K[d * S_LEN + i]; sU[i] = uT[d * S_LEN + i]; }
    __syncthreads();
    for (int s = threadIdx.x; s < S_LEN; s += blockDim.x) {
        float acc = 0.f;
        for (int l = 0; l <= s; ++l) acc += sK[l] * sU[s - l];
        yT[d * S_LEN + s] = rbf(acc);
    }
}

__device__ float block_sum(float v, float* red) {
    for (int o = 32; o >= 1; o >>= 1) v += __shfl_xor(v, o);
    __syncthreads();
    if ((threadIdx.x & 63) == 0) red[threadIdx.x >> 6] = v;
    __syncthreads();
    float t = 0.f;
    for (int i = 0; i < (int)(blockDim.x >> 6); ++i) t += red[i];
    return t;
}
__device__ float ln(float x, float* red) {
    float mean = block_sum(x, red) * (1.f / D_DIM);
    float dv = x - mean;
    float var = block_sum(dv * dv, red) * (1.f / D_DIM);
    float m = rbf(mean), v = rbf(var);
    float den = rbf(sqrtf(rbf(v + 1e-5f)));
    return rbf(rbf(x - m) / den);
}

__global__ void k_row(int mode, const u16* q, float* x, float* uT, const float* yT, const u16* Dskip,
                      const float* yAT, const float* kvT, const u16* ssmD, u16* out) {
    __shared__ float red[16];
    int s = blockIdx.x, d = threadIdx.x;
    float xv;
    if (mode == 0) xv = bf2f(q[s * D_DIM + d]);
    else {
        float u = uT[d * S_LEN + s];
        float y = rbf(yT[d * S_LEN + s] + rbf(u * bf2f(Dskip[d])));
        xv = rbf(x[s * D_DIM + d] + y);
    }
    if (mode == 2) { out[s * D_DIM + d] = f2bf(xv); return; }
    float yA = rbf(yAT[d * S_LEN + s] + rbf(kvT[d * S_LEN + s] * bf2f(ssmD[d])));
    float n = ln(xv, red);
    xv = rbf(xv + rbf(n * yA));
    float u = ln(xv, red);
    x[s * D_DIM + d] = xv;
    uT[d * S_LEN + s] = u;
}

extern "C" void kernel_launch(void* const* d_in, const int* in_sizes, int n_in,
                              void* d_out, int out_size, void* d_ws, size_t ws_size,
                              hipStream_t stream) {
    const u16* q = (const u16*)d_in[0];
    const u16* k = (const u16*)d_in[1];
    const u16* v = (const u16*)d_in[2];
    const u16* sk = (const u16*)d_in[3];
    const u16* ssmk = (const u16*)d_in[4];
    const u16* ssmD = (const u16*)d_in[5];
    const u16* logA = (const u16*)d_in[7];
    const u16* Aim = (const u16*)d_in[8];
    const u16* Cr = (const u16*)d_in[9];
    const u16* Ci = (const u16*)d_in[10];
    const u16* logdt = (const u16*)d_in[11];
    const u16* Dskip = (const u16*)d_in[12];
    const size_t NE = (size_t)S_LEN * D_DIM;
    float* ws = (float*)d_ws;
    float* kvT = ws; float* KB = ws + NE; float* KA = ws + 2 * NE; float* yAT = ws + 3 * NE;
    float* x = ws + 4 * NE; float* uT = ws + 5 * NE; float* yT = ws + 6 * NE;
    k_kv<<<NE / 256, 256, 0, stream>>>(k, v, sk, kvT);
    k_gen<<<NE / 256, 256, 0, stream>>>(logA, Aim, Cr, Ci, logdt, KB);
    k_cvt<<<NE / 256, 256, 0, stream>>>(ssmk, KA);
    k_conv<<<D_DIM, 256, 0, stream>>>(KA, kvT, yAT);
    k_row<<<S_LEN, D_DIM, 0, stream>>>(0, q, x, uT, yT, Dskip, yAT, kvT, ssmD, (u16*)d_out);
    k_conv<<<D_DIM, 256, 0, stream>>>(KB, uT, yT);
    k_row<<<S_LEN, D_DIM, 0, stream>>>(1, q, x, uT, yT, Dskip, yAT, kvT, ssmD, (u16*)d_out);
    k_conv<<<D_DIM, 256, 0, stream>>>(KB, uT, yT);
    k_row<<<S_LEN, D_DIM, 0, stream>>>(2, q, x, uT, yT, Dskip, yAT, kvT, ssmD, (u16*)d_out);
}
```

```cpp
#if defined(__HIP_DEVICE_COMPILE__)
#pragma clang attribute push(__attribute__((target("no-packed-fp32-ops"))), apply_to = function)
#endif
#include <hip/hip_runtime.h>
#include <stdint.h>

typedef unsigned short u16;
typedef unsigned u32;
typedef unsigned long long u64;
typedef u32 v4u __attribute__((ext_vector_type(4)));
typedef u32 v2u __attribute__((ext_vector_type(2)));
typedef float v4f __attribute__((ext_vector_type(4)));
typedef float v2f __attribute__((ext_vector_type(2)));
typedef __bf16 v2bf __attribute__((ext_vector_type(2)));
typedef __bf16 v8bf __attribute__((ext_vector_type(8)));
typedef _Float16 v8h __attribute__((ext_vector_type(8)));
typedef _Float16 v2h __attribute__((ext_vector_type(2)));
#define GAS __attribute__((address_space(1)))
#define LAS __attribute__((address_space(3)))
typedef GAS u64 gu64;
#define RLX_AGENT __ATOMIC_RELAXED, __HIP_MEMORY_SCOPE_AGENT

constexpr int SL = 2048, DM = 512, NST = 128;
constexpr u32 SENT = 0xFFFFFFFFu;
constexpr u32 SPIN_CAP = 1u << 15;

constexpr u32 WS_EPOCH = 0;
constexpr u32 WS_FLAG = 4096;
constexpr u32 MB1 = 1u << 20;
constexpr u32 MB2 = (2u << 20) + 65536u;
constexpr u32 WS_U = 65536u;
constexpr u32 WS_Y = WS_U + 3u * MB2;
constexpr u32 WS_BYTES = WS_U + 6u * MB2;
constexpr u32 LSPIN_CAP = 1u << 20;

constexpr int L_RR = 0;
constexpr int L_CNT = 1024;
constexpr int C_CONV = L_CNT, C_ROW = L_CNT + 64, C_Y = L_CNT + 128, C_K = L_CNT + 192, C_K1 = L_CNT + 224, L_YSC = L_CNT + 240, C_RY = L_CNT + 96, C_DUM = L_CNT + 160, C_CL = L_CNT + 32;
constexpr u32 LINC = 1u << 17;
constexpr int T_CH = 8 * 258 * 16;
constexpr int L_T = 1280;
constexpr int SCR_ROW = 528;
constexpr int SCR_CH = 57 * SCR_ROW;
constexpr int L_SCR = L_T + 2 * T_CH;
constexpr int U_CH = 320 * 16;
constexpr int L_U = L_SCR + 2 * SCR_CH;
constexpr int L_KTAB = L_U + 4 * U_CH;
constexpr int L_KL = L_KTAB + 2 * 1536;
constexpr int KL_CH = 257 * 16;
constexpr int L_TOTAL0 = L_KL + 2 * KL_CH;

constexpr int L_TOTAL = L_TOTAL0;
#define STAMP(i) do {} while (0)
#define STAMP_OUT() do {} while (0)
static_assert(L_TOTAL <= 163840, "lds");
struct Params {
    const u16 *q, *k, *v, *sk, *ssmk, *ssmD, *logA, *Aim, *Cr, *Ci, *logdt, *Dskip;
    u16* out;
    char* ws;
};

#define TLOAD_ASM \
    "ds_read_b128 a[0:3], %0 offset:4032\n\t" \
    "ds_read_b128 a[32:35], %0 offset:3520\n\t" \
    "ds_read_b128 a[64:67], %0 offset:3008\n\t" \
    "ds_read_b128 a[96:99], %0 offset:2496\n\t" \
    "ds_read_b128 a[4:7], %0 offset:3968\n\t" \
    "ds_read_b128 a[36:39], %0 offset:3456\n\t" \
    "ds_read_b128 a[68:71], %0 offset:2944\n\t" \
    "ds_read_b128 a[100:103], %0 offset:2432\n\t" \
    "ds_read_b128 a[8:11], %0 offset:3904\n\t" \
    "ds_read_b128 a[40:43], %0 offset:3392\n\t" \
    "ds_read_b128 a[72:75], %0 offset:2880\n\t" \
    "ds_read_b128 a[104:107], %0 offset:2368\n\t" \
    "ds_read_b128 a[12:15], %0 offset:3840\n\t" \
    "ds_read_b128 a[44:47], %0 offset:3328\n\t" \
    "ds_read_b128 a[76:79], %0 offset:2816\n\t" \
    "ds_read_b128 a[108:111], %0 offset:2304\n\t" \
    "ds_read_b128 a[16:19], %0 offset:3776\n\t" \
    "ds_read_b128 a[48:51], %0 offset:3264\n\t" \
    "ds_read_b128 a[80:83], %0 offset:2752\n\t" \
    "ds_read_b128 a[112:115], %0 offset:2240\n\t" \
    "ds_read_b128 a[20:23], %0 offset:3712\n\t" \
    "ds_read_b128 a[52:55], %0 offset:3200\n\t" \
    "ds_read_b128 a[84:87], %0 offset:2688\n\t" \
    "ds_read_b128 a[116:119], %0 offset:2176\n\t" \
    "ds_read_b128 a[24:27], %0 offset:3648\n\t" \
    "ds_read_b128 a[56:59], %0 offset:3136\n\t" \
    "ds_read_b128 a[88:91], %0 offset:2624\n\t" \
    "ds_read_b128 a[120:123], %0 offset:2112\n\t" \
    "ds_read_b128 a[28:31], %0 offset:3584\n\t" \
    "ds_read_b128 a[60:63], %0 offset:3072\n\t" \
    "ds_read_b128 a[92:95], %0 offset:2560\n\t" \
    "ds_read_b128 a[124:127], %0 offset:2048\n\t"
#define TLOAD_CLOB "a0", "a1", "a2", "a3", "a4", "a5", "a6", "a7", "a8", "a9", "a10", "a11", "a12", "a13", "a14", "a15", "a16", "a17", "a18", "a19", "a20", "a21", "a22", "a23", "a24", "a25", "a26", "a27", "a28", "a29", "a30", "a31", "a32", "a33", "a34", "a35", "a36", "a37", "a38", "a39", "a40", "a41", "a42", "a43", "a44", "a45", "a46", "a47", "a48", "a49", "a50", "a51", "a52", "a53", "a54", "a55", "a56", "a57", "a58", "a59", "a60", "a61", "a62", "a63", "a64", "a65", "a66", "a67", "a68", "a69", "a70", "a71", "a72", "a73", "a74", "a75", "a76", "a77", "a78", "a79", "a80", "a81", "a82", "a83", "a84", "a85", "a86", "a87", "a88", "a89", "a90", "a91", "a92", "a93", "a94", "a95", "a96", "a97", "a98", "a99", "a100", "a101", "a102", "a103", "a104", "a105", "a106", "a107", "a108", "a109", "a110", "a111", "a112", "a113", "a114", "a115", "a116", "a117", "a118", "a119", "a120", "a121", "a122", "a123", "a124", "a125", "a126", "a127"
#define CONV_CLOB "v64", "v65", "v66", "v67", "v68", "v69", "v70", "v71", "v72", "v73", "v74", "v75", "v76", "v77", "v78", "v79", "v80", "v81", "v82", "v83", "v84", "v85", "v86", "v87", "v88", "v89", "v90", "v91", "v92", "v93", "v94", "v95", "v96", "v97", "v98", "v99", "v100", "v101", "v102", "v103", "v104", "v105", "v106", "v107", "v108", "v109", "v110", "v111", "v112", "v113", "v114", "v115", "v116", "v117", "v118", "v119", "v120", "v121", "v122", "v123", "v124", "v125", "v126", "v127"
#define TRI_ASM \
    "1:\n\t" \
    "s_sub_u32 %4, %4, 1\n\t" \
    "ds_read_b32 v112, %6\n\t" \
    "ds_read_b128 v[64:67], %7 offset:1984\n\t" \
    "ds_read_b128 v[68:71], %7 offset:1472\n\t" \
    "ds_read_b128 v[72:75], %7 offset:960\n\t" \
    "ds_read_b128 v[76:79], %7 offset:448\n\t" \
    "ds_read_b128 v[80:83], %7 offset:1920\n\t" \
    "ds_read_b128 v[84:87], %7 offset:1408\n\t" \
    "ds_read_b128 v[88:91], %7 offset:896\n\t" \
    "ds_read_b128 v[92:95], %7 offset:384\n\t" \
    "ds_read_b128 v[96:99], %7 offset:1856\n\t" \
    "ds_read_b128 v[100:103], %7 offset:1344\n\t" \
    "ds_read_b128 v[104:107], %7 offset:832\n\t" \
    "ds_read_b128 v[108:111], %7 offset:320\n\t" \
    "s_waitcnt lgkmcnt(12)\n\t" \
    "v_readfirstlane_b32 %5, v112\n\t" \
    "s_cmp_lt_u32 %5, %4\n\t" \
    "s_cbranch_scc1 1b\n\t" \
    "s_waitcnt lgkmcnt(11)\n\t" \
    "v_mfma_f32_16x16x32_bf16 %2, a[0:3], v[64:67], %2\n\t" \
    "ds_read_b128 v[112:115], %7 offset:1792\n\t" \
    "s_waitcnt lgkmcnt(11)\n\t" \
    "v_mfma_f32_16x16x32_bf16 %3, a[32:35], v[68:71], %3\n\t" \
    "ds_read_b128 v[116:119], %7 offset:1280\n\t" \
    "s_waitcnt lgkmcnt(11)\n\t" \
    "v_mfma_f32_16x16x32_bf16 %0, a[0:3], v[72:75], %0\n\t" \
    "ds_read_b128 v[120:123], %7 offset:768\n\t" \
    "v_mfma_f32_16x16x32_bf16 %2, a[64:67], v[72:75], %2\n\t" \
    "ds_read_b128 v[124:127], %7 offset:256\n\t" \
    "s_waitcnt lgkmcnt(12)\n\t" \
    "v_mfma_f32_16x16x32_bf16 %1, a[32:35], v[76:79], %1\n\t" \
    "v_mfma_f32_16x16x32_bf16 %3, a[96:99], v[76:79], %3\n\t" \
    "s_waitcnt lgkmcnt(10)\n\t" \
    "v_mfma_f32_16x16x32_bf16 %2, a[4:7], v[80:83], %2\n\t" \
    "ds_read_b128 v[64:67], %7 offset:1728\n\t" \
    "v_mfma_f32_16x16x32_bf16 %3, a[36:39], v[84:87], %3\n\t" \
    "ds_read_b128 v[68:71], %7 offset:1216\n\t" \
    "s_waitcnt lgkmcnt(10)\n\t" \
    "v_mfma_f32_16x16x32_bf16 %0, a[4:7], v[88:91], %0\n\t" \
    "ds_read_b128 v[72:75], %7 offset:704\n\t" \
    "v_mfma_f32_16x16x32_bf16 %2, a[68:71], v[88:91], %2\n\t" \
    "ds_read_b128 v[76:79], %7 offset:192\n\t" \
    "v_mfma_f32_16x16x32_bf16 %1, a[36:39], v[92:95], %1\n\t" \
    "v_mfma_f32_16x16x32_bf16 %3, a[100:103], v[92:95], %3\n\t" \
    "s_waitcnt lgkmcnt(10)\n\t" \
    "v_mfma_f32_16x16x32_bf16 %2, a[8:11], v[96:99], %2\n\t" \
    "ds_read_b128 v[80:83], %7 offset:1664\n\t" \
    "v_mfma_f32_16x16x32_bf16 %3, a[40:43], v[100:103], %3\n\t" \
    "ds_read_b128 v[84:87], %7 offset:1152\n\t" \
    "s_waitcnt lgkmcnt(10)\n\t" \
    "v_mfma_f32_16x16x32_bf16 %0, a[8:11], v[104:107], %0\n\t" \
    "ds_read_b128 v[88:91], %7 offset:640\n\t" \
    "v_mfma_f32_16x16x32_bf16 %2, a[72:75], v[104:107], %2\n\t" \
    "ds_read_b128 v[92:95], %7 offset:128\n\t" \
    "v_mfma_f32_16x16x32_bf16 %1, a[40:43], v[108:111], %1\n\t" \
    "v_mfma_f32_16x16x32_bf16 %3, a[104:107], v[108:111], %3\n\t" \
    "s_waitcnt lgkmcnt(10)\n\t" \
    "v_mfma_f32_16x16x32_bf16 %2, a[12:15], v[112:115], %2\n\t" \
    "ds_read_b128 v[96:99], %7 offset:1600\n\t" \
    "v_mfma_f32_16x16x32_bf16 %3, a[44:47], v[116:119], %3\n\t" \
    "ds_read_b128 v[100:103], %7 offset:1088\n\t" \
    "s_waitcnt lgkmcnt(10)\n\t" \
    "v_mfma_f32_16x16x32_bf16 %0, a[12:15], v[120:123], %0\n\t" \
    "ds_read_b128 v[104:107], %7 offset:576\n\t" \
    "v_mfma_f32_16x16x32_bf16 %2, a[76:79], v[120:123], %2\n\t" \
    "ds_read_b128 v[108:111], %7 offset:64\n\t" \
    "v_mfma_f32_16x16x32_bf16 %1, a[44:47], v[124:127], %1\n\t" \
    "v_mfma_f32_16x16x32_bf16 %3, a[108:111], v[124:127], %3\n\t" \
    "s_waitcnt lgkmcnt(10)\n\t" \
    "v_mfma_f32_16x16x32_bf16 %2, a[16:19], v[64:67], %2\n\t" \
    "ds_read_b128 v[112:115], %7 offset:1536\n\t" \
    "v_mfma_f32_16x16x32_bf16 %3, a[48:51], v[68:71], %3\n\t" \
    "ds_read_b128 v[116:119], %7 offset:1024\n\t" \
    "s_waitcnt lgkmcnt(10)\n\t" \
    "v_mfma_f32_16x16x32_bf16 %0, a[16:19], v[72:75], %0\n\t" \
    "ds_read_b128 v[120:123], %7 offset:512\n\t" \
    "v_mfma_f32_16x16x32_bf16 %2, a[80:83], v[72:75], %2\n\t" \
    "ds_read_b128 v[124:127], %7 offset:0\n\t" \
    "v_mfma_f32_16x16x32_bf16 %1, a[48:51], v[76:79], %1\n\t" \
    "v_mfma_f32_16x16x32_bf16 %3, a[112:115], v[76:79], %3\n\t" \
    "s_waitcnt lgkmcnt(10)\n\t" \
    "v_mfma_f32_16x16x32_bf16 %2, a[20:23], v[80:83], %2\n\t" \
    "v_mfma_f32_16x16x32_bf16 %3, a[52:55], v[84:87], %3\n\t" \
    "s_waitcnt lgkmcnt(8)\n\t" \
    "v_mfma_f32_16x16x32_bf16 %0, a[20:23], v[88:91], %0\n\t" \
    "v_mfma_f32_16x16x32_bf16 %2, a[84:87], v[88:91], %2\n\t" \
    "v_mfma_f32_16x16x32_bf16 %1, a[52:55], v[92:95], %1\n\t" \
    "v_mfma_f32_16x16x32_bf16 %3, a[116:119], v[92:95], %3\n\t" \
    "s_waitcnt lgkmcnt(6)\n\t" \
    "v_mfma_f32_16x16x32_bf16 %2, a[24:27], v[96:99], %2\n\t" \
    "v_mfma_f32_16x16x32_bf16 %3, a[56:59], v[100:103], %3\n\t" \
    "s_waitcnt lgkmcnt(4)\n\t" \
    "v_mfma_f32_16x16x32_bf16 %0, a[24:27], v[104:107], %0\n\t" \
    "v_mfma_f32_16x16x32_bf16 %2, a[88:91], v[104:107], %2\n\t" \
    "v_mfma_f32_16x16x32_bf16 %1, a[56:59], v[108:111], %1\n\t" \
    "v_mfma_f32_16x16x32_bf16 %3, a[120:123], v[108:111], %3\n\t" \
    "s_waitcnt lgkmcnt(2)\n\t" \
    "v_mfma_f32_16x16x32_bf16 %2, a[28:31], v[112:115], %2\n\t" \
    "v_mfma_f32_16x16x32_bf16 %3, a[60:63], v[116:119], %3\n\t" \
    "s_waitcnt lgkmcnt(0)\n\t" \
    "v_mfma_f32_16x16x32_bf16 %0, a[28:31], v[120:123], %0\n\t" \
    "v_mfma_f32_16x16x32_bf16 %2, a[92:95], v[120:123], %2\n\t" \
    "v_mfma_f32_16x16x32_bf16 %1, a[60:63], v[124:127], %1\n\t" \
    "v_mfma_f32_16x16x32_bf16 %3, a[124:127], v[124:127], %3\n\t" \
    "s_nop 7\n\t" \
    "s_nop 1\n\t"
#define CROSS_ASM \
    "ds_read_b128 v[64:67], %4 offset:1984\n\t" \
    "ds_read_b128 v[68:71], %4 offset:1472\n\t" \
    "ds_read_b128 v[72:75], %5 offset:1984\n\t" \
    "ds_read_b128 v[76:79], %5 offset:1472\n\t" \
    "ds_read_b128 v[80:83], %4 offset:960\n\t" \
    "ds_read_b128 v[84:87], %4 offset:448\n\t" \
    "ds_read_b128 v[88:91], %5 offset:960\n\t" \
    "ds_read_b128 v[92:95], %5 offset:448\n\t" \
    "ds_read_b128 v[96:99], %4 offset:1920\n\t" \
    "ds_read_b128 v[100:103], %4 offset:1408\n\t" \
    "ds_read_b128 v[104:107], %5 offset:1920\n\t" \
    "ds_read_b128 v[108:111], %5 offset:1408\n\t" \
    "ds_read_b128 v[112:115], %4 offset:896\n\t" \
    "s_waitcnt lgkmcnt(11)\n\t" \
    "v_mfma_f32_16x16x32_bf16 %0, a[64:67], v[64:67], %0\n\t" \
    "ds_read_b128 v[116:119], %4 offset:384\n\t" \
    "v_mfma_f32_16x16x32_bf16 %1, a[96:99], v[68:71], %1\n\t" \
    "ds_read_b128 v[120:123], %5 offset:896\n\t" \
    "s_waitcnt lgkmcnt(11)\n\t" \
    "v_mfma_f32_16x16x32_bf16 %2, v[72:75], v[64:67], %2\n\t" \
    "ds_read_b128 v[124:127], %5 offset:384\n\t" \
    "v_mfma_f32_16x16x32_bf16 %3, v[76:79], v[68:71], %3\n\t" \
    "ds_read_b128 v[64:67], %4 offset:1856\n\t" \
    "s_waitcnt lgkmcnt(11)\n\t" \
    "v_mfma_f32_16x16x32_bf16 %0, v[72:75], v[80:83], %0\n\t" \
    "ds_read_b128 v[68:71], %4 offset:1344\n\t" \
    "v_mfma_f32_16x16x32_bf16 %1, v[76:79], v[84:87], %1\n\t" \
    "ds_read_b128 v[72:75], %5 offset:1856\n\t" \
    "s_waitcnt lgkmcnt(11)\n\t" \
    "v_mfma_f32_16x16x32_bf16 %2, v[88:91], v[80:83], %2\n\t" \
    "ds_read_b128 v[76:79], %5 offset:1344\n\t" \
    "v_mfma_f32_16x16x32_bf16 %3, v[92:95], v[84:87], %3\n\t" \
    "ds_read_b128 v[80:83], %4 offset:832\n\t" \
    "s_waitcnt lgkmcnt(11)\n\t" \
    "v_mfma_f32_16x16x32_bf16 %0, a[68:71], v[96:99], %0\n\t" \
    "ds_read_b128 v[84:87], %4 offset:320\n\t" \
    "v_mfma_f32_16x16x32_bf16 %1, a[100:103], v[100:103], %1\n\t" \
    "ds_read_b128 v[88:91], %5 offset:832\n\t" \
    "s_waitcnt lgkmcnt(11)\n\t" \
    "v_mfma_f32_16x16x32_bf16 %2, v[104:107], v[96:99], %2\n\t" \
    "ds_read_b128 v[92:95], %5 offset:320\n\t" \
    "v_mfma_f32_16x16x32_bf16 %3, v[108:111], v[100:103], %3\n\t" \
    "ds_read_b128 v[96:99], %4 offset:1792\n\t" \
    "s_waitcnt lgkmcnt(11)\n\t" \
    "v_mfma_f32_16x16x32_bf16 %0, v[104:107], v[112:115], %0\n\t" \
    "ds_read_b128 v[100:103], %4 offset:1280\n\t" \
    "v_mfma_f32_16x16x32_bf16 %1, v[108:111], v[116:119], %1\n\t" \
    "ds_read_b128 v[104:107], %5 offset:1792\n\t" \
    "s_waitcnt lgkmcnt(11)\n\t" \
    "v_mfma_f32_16x16x32_bf16 %2, v[120:123], v[112:115], %2\n\t" \
    "ds_read_b128 v[108:111], %5 offset:1280\n\t" \
    "v_mfma_f32_16x16x32_bf16 %3, v[124:127], v[116:119], %3\n\t" \
    "ds_read_b128 v[112:115], %4 offset:768\n\t" \
    "s_waitcnt lgkmcnt(11)\n\t" \
    "v_mfma_f32_16x16x32_bf16 %0, a[72:75], v[64:67], %0\n\t" \
    "ds_read_b128 v[116:119], %4 offset:256\n\t" \
    "v_mfma_f32_16x16x32_bf16 %1, a[104:107], v[68:71], %1\n\t" \
    "ds_read_b128 v[120:123], %5 offset:768\n\t" \
    "s_waitcnt lgkmcnt(11)\n\t" \
    "v_mfma_f32_16x16x32_bf16 %2, v[72:75], v[64:67], %2\n\t" \
    "ds_read_b128 v[124:127], %5 offset:256\n\t" \
    "v_mfma_f32_16x16x32_bf16 %3, v[76:79], v[68:71], %3\n\t" \
    "ds_read_b128 v[64:67], %4 offset:1728\n\t" \
    "s_waitcnt lgkmcnt(11)\n\t" \
    "v_mfma_f32_16x16x32_bf16 %0, v[72:75], v[80:83], %0\n\t" \
    "ds_read_b128 v[68:71], %4 offset:1216\n\t" \
    "v_mfma_f32_16x16x32_bf16 %1, v[76:79], v[84:87], %1\n\t" \
    "ds_read_b128 v[72:75], %5 offset:1728\n\t" \
    "s_waitcnt lgkmcnt(11)\n\t" \
    "v_mfma_f32_16x16x32_bf16 %2, v[88:91], v[80:83], %2\n\t" \
    "ds_read_b128 v[76:79], %5 offset:1216\n\t" \
    "v_mfma_f32_16x16x32_bf16 %3, v[92:95], v[84:87], %3\n\t" \
    "ds_read_b128 v[80:83], %4 offset:704\n\t" \
    "s_waitcnt lgkmcnt(11)\n\t" \
    "v_mfma_f32_16x16x32_bf16 %0, a[76:79], v[96:99], %0\n\t" \
    "ds_read_b128 v[84:87], %4 offset:192\n\t" \
    "v_mfma_f32_16x16x32_bf16 %1, a[108:111], v[100:103], %1\n\t" \
    "ds_read_b128 v[88:91], %5 offset:704\n\t" \
    "s_waitcnt lgkmcnt(11)\n\t" \
    "v_mfma_f32_16x16x32_bf16 %2, v[104:107], v[96:99], %2\n\t" \
    "ds_read_b128 v[92:95], %5 offset:192\n\t" \
    "v_mfma_f32_16x16x32_bf16 %3, v[108:111], v[100:103], %3\n\t" \
    "ds_read_b128 v[96:99], %4 offset:1664\n\t" \
    "s_waitcnt lgkmcnt(11)\n\t" \
    "v_mfma_f32_16x16x32_bf16 %0, v[104:107], v[112:115], %0\n\t" \
    "ds_read_b128 v[100:103], %4 offset:1152\n\t" \
    "v_mfma_f32_16x16x32_bf16 %1, v[108:111], v[116:119], %1\n\t" \
    "ds_read_b128 v[104:107], %5 offset:1664\n\t" \
    "s_waitcnt lgkmcnt(11)\n\t" \
    "v_mfma_f32_16x16x32_bf16 %2, v[120:123], v[112:115], %2\n\t" \
    "ds_read_b128 v[108:111], %5 offset:1152\n\t" \
    "v_mfma_f32_16x16x32_bf16 %3, v[124:127], v[116:119], %3\n\t" \
    "ds_read_b128 v[112:115], %4 offset:640\n\t" \
    "s_waitcnt lgkmcnt(11)\n\t" \
    "v_mfma_f32_16x16x32_bf16 %0, a[80:83], v[64:67], %0\n\t" \
    "ds_read_b128 v[116:119], %4 offset:128\n\t" \
    "v_mfma_f32_16x16x32_bf16 %1, a[112:115], v[68:71], %1\n\t" \
    "ds_read_b128 v[120:123], %5 offset:640\n\t" \
    "s_waitcnt lgkmcnt(11)\n\t" \
    "v_mfma_f32_16x16x32_bf16 %2, v[72:75], v[64:67], %2\n\t" \
    "ds_read_b128 v[124:127], %5 offset:128\n\t" \
    "v_mfma_f32_16x16x32_bf16 %3, v[76:79], v[68:71], %3\n\t" \
    "ds_read_b128 v[64:67], %4 offset:1600\n\t" \
    "s_waitcnt lgkmcnt(11)\n\t" \
    "v_mfma_f32_16x16x32_bf16 %0, v[72:75], v[80:83], %0\n\t" \
    "ds_read_b128 v[68:71], %4 offset:1088\n\t" \
    "v_mfma_f32_16x16x32_bf16 %1, v[76:79], v[84:87], %1\n\t" \
    "ds_read_b128 v[72:75], %5 offset:1600\n\t" \
    "s_waitcnt lgkmcnt(11)\n\t" \
    "v_mfma_f32_16x16x32_bf16 %2, v[88:91], v[80:83], %2\n\t" \
    "ds_read_b128 v[76:79], %5 offset:1088\n\t" \
    "v_mfma_f32_16x16x32_bf16 %3, v[92:95], v[84:87], %3\n\t" \
    "ds_read_b128 v[80:83], %4 offset:576\n\t" \
    "s_waitcnt lgkmcnt(11)\n\t" \
    "v_mfma_f32_16x16x32_bf16 %0, a[84:87], v[96:99], %0\n\t" \
    "ds_read_b128 v[84:87], %4 offset:64\n\t" \
    "v_mfma_f32_16x16x32_bf16 %1, a[116:119], v[100:103], %1\n\t" \
    "ds_read_b128 v[88:91], %5 offset:576\n\t" \
    "s_waitcnt lgkmcnt(11)\n\t" \
    "v_mfma_f32_16x16x32_bf16 %2, v[104:107], v[96:99], %2\n\t" \
    "ds_read_b128 v[92:95], %5 offset:64\n\t" \
    "v_mfma_f32_16x16x32_bf16 %3, v[108:111], v[100:103], %3\n\t" \
    "ds_read_b128 v[96:99], %4 offset:1536\n\t" \
    "s_waitcnt lgkmcnt(11)\n\t" \
    "v_mfma_f32_16x16x32_bf16 %0, v[104:107], v[112:115], %0\n\t" \
    "ds_read_b128 v[100:103], %4 offset:1024\n\t" \
    "v_mfma_f32_16x16x32_bf16 %1, v[108:111], v[116:119], %1\n\t" \
    "ds_read_b128 v[104:107], %5 offset:1536\n\t" \
    "s_waitcnt lgkmcnt(11)\n\t" \
    "v_mfma_f32_16x16x32_bf16 %2, v[120:123], v[112:115], %2\n\t" \
    "ds_read_b128 v[108:111], %5 offset:1024\n\t" \
    "v_mfma_f32_16x16x32_bf16 %3, v[124:127], v[116:119], %3\n\t" \
    "ds_read_b128 v[112:115], %4 offset:512\n\t" \
    "s_waitcnt lgkmcnt(11)\n\t" \
    "v_mfma_f32_16x16x32_bf16 %0, a[88:91], v[64:67], %0\n\t" \
    "ds_read_b128 v[116:119], %4 offset:0\n\t" \
    "v_mfma_f32_16x16x32_bf16 %1, a[120:123], v[68:71], %1\n\t" \
    "ds_read_b128 v[120:123], %5 offset:512\n\t" \
    "s_waitcnt lgkmcnt(11)\n\t" \
    "v_mfma_f32_16x16x32_bf16 %2, v[72:75], v[64:67], %2\n\t" \
    "ds_read_b128 v[124:127], %5 offset:0\n\t" \
    "v_mfma_f32_16x16x32_bf16 %3, v[76:79], v[68:71], %3\n\t" \
    "s_waitcnt lgkmcnt(10)\n\t" \
    "v_mfma_f32_16x16x32_bf16 %0, v[72:75], v[80:83], %0\n\t" \
    "v_mfma_f32_16x16x32_bf16 %1, v[76:79], v[84:87], %1\n\t" \
    "s_waitcnt lgkmcnt(8)\n\t" \
    "v_mfma_f32_16x16x32_bf16 %2, v[88:91], v[80:83], %2\n\t" \
    "v_mfma_f32_16x16x32_bf16 %3, v[92:95], v[84:87], %3\n\t" \
    "s_waitcnt lgkmcnt(6)\n\t" \
    "v_mfma_f32_16x16x32_bf16 %0, a[92:95], v[96:99], %0\n\t" \
    "v_mfma_f32_16x16x32_bf16 %1, a[124:127], v[100:103], %1\n\t" \
    "s_waitcnt lgkmcnt(4)\n\t" \
    "v_mfma_f32_16x16x32_bf16 %2, v[104:107], v[96:99], %2\n\t" \
    "v_mfma_f32_16x16x32_bf16 %3, v[108:111], v[100:103], %3\n\t" \
    "s_waitcnt lgkmcnt(2)\n\t" \
    "v_mfma_f32_16x16x32_bf16 %0, v[104:107], v[112:115], %0\n\t" \
    "v_mfma_f32_16x16x32_bf16 %1, v[108:111], v[116:119], %1\n\t" \
    "s_waitcnt lgkmcnt(0)\n\t" \
    "v_mfma_f32_16x16x32_bf16 %2, v[120:123], v[112:115], %2\n\t" \
    "v_mfma_f32_16x16x32_bf16 %3, v[124:127], v[116:119], %3\n\t" \
    "s_nop 7\n\t" \
    "s_nop 1\n\t"

__device__ __forceinline__ float u2f(u32 x) { return __builtin_bit_cast(float, x); }
__device__ __forceinline__ u32 f2u(float x) { return __builtin_bit_cast(u32, x); }
__device__ __forceinline__ float blo(u32 x) { return u2f(x << 16); }
__device__ __forceinline__ float bhi(u32 x) { return u2f(x & 0xffff0000u); }
__device__ __forceinline__ u32 pkbf(float a, float b) {
    v2f f = {a, b};
    return __builtin_bit_cast(u32, __builtin_convertvector(f, v2bf));
}
__device__ __forceinline__ float rbf(float a) { return blo(pkbf(a, a)); }
__device__ __forceinline__ u32 pkh(float a, float b) {
    v2f f = {a, b};
    return __builtin_bit_cast(u32, __builtin_convertvector(f, v2h));
}
template <int CTRL> __device__ __forceinline__ float dppf(float x) {
    return __builtin_bit_cast(float, __builtin_amdgcn_mov_dpp(__builtin_bit_cast(int, x), CTRL, 0xf, 0xf, true));
}
template <int CTRL> __device__ __forceinline__ u32 dppu(u32 x) {
    return (u32)__builtin_amdgcn_mov_dpp((int)x, CTRL, 0xf, 0xf, true);
}
constexpr int XOR1 = 0xB1, XOR2 = 0x4E, HMIR = 0x141, MIR = 0x140;
__device__ __forceinline__ void bar() { asm volatile("s_waitcnt lgkmcnt(0)\n\ts_barrier" ::: "memory"); }
__device__ __forceinline__ void wait_vm0() { asm volatile("s_waitcnt vmcnt(0)" ::: "memory"); }
__device__ __forceinline__ u32 umin(u32 a, u32 b) { return a < b ? a : b; }
__device__ __forceinline__ u32 umax(u32 a, u32 b) { return a > b ? a : b; }
__device__ __forceinline__ u32 ns(u32 x) { return umin(x, SENT - 1u); }
__device__ __forceinline__ bool isvalid(v4u x) { return umax(umax(x.x, x.y), umax(x.z, x.w)) != SENT; }
template <class T> __device__ __forceinline__ void pin(T& x) { asm volatile("" : "+v"(x)); }

typedef __amdgpu_buffer_rsrc_t rsrc_t;
__device__ __forceinline__ rsrc_t mkr(const void* p, u32 bytes) { return __builtin_amdgcn_make_buffer_rsrc((void*)p, 0, bytes, 0x00020000); }
__device__ __forceinline__ void st16(rsrc_t r, u32 off, u32 so, v4u v) {
    asm volatile("" : "+v"(v));
    __builtin_amdgcn_raw_buffer_store_b128(v, r, off, so, 16);
    asm volatile("s_nop 1" :: "v"(v));
}
__device__ __forceinline__ void st8(rsrc_t r, u32 off, u32 so, v2u v) { __builtin_amdgcn_raw_buffer_store_b64(v, r, off, so, 16); }
__device__ __forceinline__ v4u ld16(rsrc_t r, u32 off, u32 so) { return __builtin_amdgcn_raw_buffer_load_b128(r, off, so, 16); }
__device__ __forceinline__ u32 ldw(rsrc_t r, u32 off, u32 so) { return __builtin_amdgcn_raw_buffer_load_b32(r, off, so, 0); }
__device__ __forceinline__ u32 ldh(rsrc_t r, u32 off, u32 so) { return (u32)__builtin_amdgcn_raw_buffer_load_b16(r, off, so, 0); }

__device__ __forceinline__ void wait_flag(rsrc_t r, u32 off, v2u tag, bool& dead) {
    u32 spins = 0;
    while (!dead) {
        asm volatile("" ::: "memory");
        const v2u x = __builtin_amdgcn_raw_buffer_load_b64(r, off, 0, 16);
        if (x.x == tag.x && x.y == tag.y) break;
        if (++spins > SPIN_CAP) dead = true;
    }
}
__device__ __forceinline__ void repoll16(rsrc_t r, u32 off, u32 so, v4u& x, bool& dead) {
    u32 spins = 0;
    while (!isvalid(x) && !dead) {
        asm volatile("" ::: "memory");
        x = ld16(r, off, so);
        if (++spins > SPIN_CAP) dead = true;
    }
}

__device__ __forceinline__ void build_T(char* base, v4u lo, v4u hi) {
    const u32 W[8] = {lo.x, lo.y, lo.z, lo.w, hi.x, hi.y, hi.z, hi.w};
    u32 R[8], B[8];
#pragma unroll
    for (int m = 1; m < 8; ++m) {
        R[m] = __builtin_amdgcn_alignbit(W[m], W[m], 16);
        B[m] = (W[m] & 0xffffu) | (W[m - 1] & 0xffff0000u);
    }
#pragma unroll
    for (int rho = 0; rho < 8; ++rho) {
        const int idx = rho < 4 ? 2 * rho : 2 * (rho - 4) + 1;
        v4u o;
        if (rho & 1) { const int m = (7 + rho) / 2; o = (v4u){R[m], R[m - 1], R[m - 2], R[m - 3]}; }
        else { const int m = 4 + rho / 2; o = (v4u){B[m], B[m - 1], B[m - 2], B[m - 3]}; }
        *(v4u*)(base + idx * 258 * 16) = o;
    }
}

__device__ __forceinline__ void to_rowlay(v4u a, u32 (&o)[4]) {
    auto s0 = __builtin_amdgcn_permlane32_swap(a.x, a.z, false, false);
    auto s1 = __builtin_amdgcn_permlane32_swap(a.y, a.w, false, false);
    auto s2 = __builtin_amdgcn_permlane16_swap(s0[0], s1[0], false, false);
    auto s3 = __builtin_amdgcn_permlane16_swap(s0[1], s1[1], false, false);
    o[0] = s2[0]; o[1] = s2[1]; o[2] = s3[0]; o[3] = s3[1];
}
__device__ __forceinline__ v4u from_rowlay(const u32 (&o)[4]) {
    auto s2 = __builtin_amdgcn_permlane16_swap(o[0], o[1], false, false);
    auto s3 = __builtin_amdgcn_permlane16_swap(o[2], o[3], false, false);
    auto s0 = __builtin_amdgcn_permlane32_swap(s2[0], s3[0], false, false);
    auto s1 = __builtin_amdgcn_permlane32_swap(s2[1], s3[1], false, false);
    return (v4u){s0[0], s1[0], s0[1], s1[1]};
}

__device__ __forceinline__ void lsig(u32 addr) {
    asm volatile("s_mov_b64 exec, 1\n\tds_add_u32 %0, %1\n\ts_mov_b64 exec, -1" :: "v"(addr), "v"(LINC) : "memory");
}
#define SETPRIO(x) __builtin_amdgcn_s_setprio(x)
__device__ __forceinline__ void lwait(const char* p, u32 target) {
    u32 tg = target * LINC + 1u;
    do {
        asm volatile("" ::: "memory");
        --tg;
    } while ((u32)__builtin_amdgcn_readfirstlane(*(const u32*)p) < tg);
    asm volatile("" ::: "memory");
}
__device__ __forceinline__ bool anybad(v4u a, v4u b) {
    return __builtin_amdgcn_ballot_w64(umax(umax(umax(a.x, a.y), umax(a.z, a.w)), umax(umax(b.x, b.y), umax(b.z, b.w))) == SENT) != 0;
}
__device__ __forceinline__ void poll2(rsrc_t ws, u32 off, u32 so0, u32 so1, v4u& a, v4u& b, bool& dead) {
    a = ld16(ws, off, so0); b = ld16(ws, off, so1);
    u32 spins = dead ? SPIN_CAP : 0;
    while (anybad(a, b)) {
        if (++spins > SPIN_CAP) { dead = true; break; }
        asm volatile("" ::: "memory");
        if (!isvalid(a)) a = ld16(ws, off, so0);
        if (!isvalid(b)) b = ld16(ws, off, so1);
    }
}
__device__ __forceinline__ void poll1(rsrc_t ws, u32 off, u32 so, v4u& a, bool& dead) {
    a = ld16(ws, off, so);
    u32 spins = dead ? SPIN_CAP : 0;
    while (__builtin_amdgcn_ballot_w64(!isvalid(a)) != 0) {
        if (++spins > SPIN_CAP) { dead = true; break; }
        asm volatile("" ::: "memory");
        if (!isvalid(a)) a = ld16(ws, off, so);
    }
}
__device__ __forceinline__ void pollf(rsrc_t ws, u32 off, u32 so, v2u& f, v2u tag, bool& dead) {
    u32 spins = dead ? SPIN_CAP : 0;
    while (__builtin_amdgcn_ballot_w64(f.x != tag.x || f.y != tag.y) != 0) {
        if (++spins > SPIN_CAP) { dead = true; break; }
        asm volatile("" ::: "memory");
        f = __builtin_amdgcn_raw_buffer_load_b64(ws, off, so, 16);
    }
}

struct Stat { float ma, mb, ra, rb; };
__device__ __forceinline__ void row_stats(char* rr, int r, int lane, const float (&da)[8], const float (&db)[8], Stat& s, float& na, float& nb) {
    float v0 = ((da[0] + da[1]) + (da[2] + da[3])) + ((da[4] + da[5]) + (da[6] + da[7]));
    float v2 = ((db[0] + db[1]) + (db[2] + db[3])) + ((db[4] + db[5]) + (db[6] + db[7]));
    float v1 = da[0] * da[0], v1b = da[1] * da[1], v3 = db[0] * db[0], v3b = db[1] * db[1];
#pragma unroll
    for (int k = 2; k < 8; k += 2) {
        v1 = __builtin_fmaf(da[k], da[k], v1); v1b = __builtin_fmaf(da[k + 1], da[k + 1], v1b);
        v3 = __builtin_fmaf(db[k], db[k], v3); v3b = __builtin_fmaf(db[k + 1], db[k + 1], v3b);
    }
    v1 += v1b; v3 += v3b;
    asm volatile(
        "s_nop 1\n\t"
        "v_add_f32_dpp %0, %0, %0 row_ror:8 row_mask:0xf bank_mask:0x3\n\t"
        "v_add_f32_dpp %1, %1, %1 row_ror:8 row_mask:0xf bank_mask:0x3\n\t"
        "v_add_f32_dpp %0, %2, %2 row_ror:8 row_mask:0xf bank_mask:0xc\n\t"
        "v_add_f32_dpp %1, %3, %3 row_ror:8 row_mask:0xf bank_mask:0xc\n\t"
        "s_nop 0\n\t"
        "v_add_f32_dpp %0, %0, %0 row_half_mirror row_mask:0xf bank_mask:0x5\n\t"
        "v_add_f32_dpp %0, %1, %1 row_half_mirror row_mask:0xf bank_mask:0xa\n\t"
        "s_nop 1\n\t"
        "v_add_f32_dpp %0, %0, %0 quad_perm:[1,0,3,2] row_mask:0xf bank_mask:0xf\n\t"
        "s_nop 1\n\t"
        "v_add_f32_dpp %0, %0, %0 quad_perm:[2,3,0,1] row_mask:0xf bank_mask:0xf\n\t"
        : "+v"(v0), "+v"(v1) : "v"(v2), "v"(v3));
    *(u32*)(rr + (lane >> 4) * 64 + r * 16 + (lane & 12)) = ns(f2u(v0));
    v4u zi;
    u32 spins = 0;
    do {
        asm volatile("" ::: "memory");
        zi = *(const v4u*)(rr + (lane >> 4) * 64 + (lane & 3) * 16);
    } while (__builtin_amdgcn_ballot_w64(!isvalid(zi)) != 0 && ++spins < LSPIN_CAP);
    v4f z = __builtin_bit_cast(v4f, zi);
#pragma unroll
    for (int k = 0; k < 4; ++k) z[k] += dppf<XOR1>(z[k]);
#pragma unroll
    for (int k = 0; k < 4; ++k) z[k] += dppf<XOR2>(z[k]);
    const float inv = 1.f / DM;
    const float mua = z[0] * inv, mub = z[2] * inv;
    const float va = __builtin_fmaxf(__builtin_fmaf(-mua, mua, z[1] * inv), 0.f), vb = __builtin_fmaxf(__builtin_fmaf(-mub, mub, z[3] * inv), 0.f);
    const u32 pv = pkbf(va, vb);
    const u32 pe = pkbf(blo(pv) + 1e-5f, bhi(pv) + 1e-5f);
    const u32 pd = pkbf(__builtin_amdgcn_sqrtf(blo(pe)), __builtin_amdgcn_sqrtf(bhi(pe)));
    const u32 pm = pkbf(s.ma + mua, s.mb + mub);
    s.ra = __builtin_amdgcn_rcpf(blo(pd));
    s.rb = __builtin_amdgcn_rcpf(bhi(pd));
    na = (s.ma - blo(pm)) * s.ra;
    nb = (s.mb - bhi(pm)) * s.rb;
    s.ma = blo(pm);
    s.mb = bhi(pm);
}
#define MFMA(a, b, c) __builtin_amdgcn_mfma_f32_16x16x32_bf16(a, b, c, 0, 0, 0)
#define SGB(m, n) __builtin_amdgcn_sched_group_barrier(m, n, 0)
__device__ __forceinline__ u32 ln2(u32 x, const Stat& s) {
    const u32 pd = pkbf(blo(x) - s.ma, bhi(x) - s.mb);
    return pkbf(blo(pd) * s.ra, bhi(pd) * s.rb);
}
__device__ __forceinline__ u32 add2(u32 a, u32 b) { return pkbf(blo(a) + blo(b), bhi(a) + bhi(b)); }
__device__ __forceinline__ u32 mul2(u32 a, u32 b) { return pkbf(blo(a) * blo(b), bhi(a) * bhi(b)); }
__device__ __forceinline__ v4u pair_xpose(bool odd, u32 c0a, u32 c0b, u32 c1a, u32 c1b) {
    const u32 sa = odd ? c0a : c1a, sb = odd ? c0b : c1b;
    const u32 ra = dppu<XOR1>(sa), rb = dppu<XOR1>(sb);
    return (v4u){odd ? ra : c0a, odd ? rb : c0b, odd ? c1a : ra, odd ? c1b : rb};
}

struct cf { float r, i; };
__device__ __forceinline__ cf cmul(cf a, cf b) { return {__builtin_fmaf(a.r, b.r, -(a.i * b.i)), __builtin_fmaf(a.r, b.i, a.i * b.r)}; }
template <int K> __device__ __forceinline__ cf cexpk(float la, float th, float tl) {
    const float sc = (float)(1 << K);
    const float mag = __builtin_amdgcn_exp2f(la * sc);
    const float ph = __builtin_fmaf(tl, sc, __builtin_amdgcn_fractf(th * sc));
    return {mag * __builtin_amdgcn_cosf(ph), mag * __builtin_amdgcn_sinf(ph)};
}
__device__ __forceinline__ float exp_acc(float x) {
    const float y = x * 1.44269504088896340736f;
    const float n = __builtin_rintf(y);
    float f = __builtin_fmaf(x, 1.44269504088896340736f, -n);
    f = __builtin_fmaf(x, 1.92596299e-8f, f);
    const float nc = __builtin_fminf(__builtin_fmaxf(n, -300.f), 300.f);
    const float e = __builtin_ldexpf(__builtin_amdgcn_exp2f(f), (int)nc);
    return __builtin_fabsf(y) < 400.f ? e : __builtin_amdgcn_exp2f(y);
}

#define DLY_U0 200
#define DLY_U1 300
#define DLY_Y 14
#define DLY_K 6
#define DLY_S 4
#define CODE_BYTES 27648
#define SSMK_POS 0
#define Q_POS 1
#define YC_POS 1
#define CPRIO 1
#define RPRIO 0
struct KG { float la, th, tl, dt; };
struct KP { u32 a, im, dt; };
__device__ __forceinline__ KP kg_load(const Params& p, int chan, int kn) {
    KP k;
    const u32 so = (u32)chan * (NST * 2);
    k.a = ldh(mkr(p.logA, DM * NST * 2), kn * 2, so);
    k.dt = ldh(mkr(p.logdt, DM * 2), 0, chan * 2);
    k.im = ldh(mkr(p.Aim, DM * NST * 2), kn * 2, so);
    return k;
}
__device__ __forceinline__ KG kg_prep(const KP& k) {
    KG g;
    g.dt = rbf(exp_acc(blo(k.dt)));
    const float zr = -rbf(exp_acc(blo(k.a))) * g.dt;
    const float zi = blo(k.im) * g.dt;
    g.la = zr * 1.44269504088896340736f;
    g.th = zi * 0.15915494309189533577f;
    g.tl = __builtin_fmaf(zi, 6.4206383e-9f, __builtin_fmaf(zi, 0.15915494309189533577f, -g.th));
    return g;
}
#define FENCE() asm volatile("" ::: "memory")
#define PUTH(row, h) do { const cf h_ = (h); *(u32*)(scr + (row) * SCR_ROW) = pkh(h_.r, -h_.i); } while (0)
#define PUTL(row, l) do { const cf l_ = (l); *(u32*)(scr + (41 + (row)) * SCR_ROW) = pkh(l_.r, l_.i); } while (0)
#define CEXP(k) cexpk<k>(g.la, g.th, g.tl)
#define PUTP(row, v) (*(u32*)(scr + (row) * SCR_ROW) = (v))
__device__ __forceinline__ u32 pkc(cf a) { return pkh(a.r, -a.i); }
__device__ __forceinline__ u32 pkp(cf a) { return pkh(a.r, a.i); }
__device__ __forceinline__ u32 hmul(u32 x, u32 y) {
    u32 t;
    asm("v_pk_mul_f16 %0, %1, %2 op_sel:[0,0] op_sel_hi:[0,1]\n\t"
        "v_pk_fma_f16 %0, %1, %2, %0 op_sel:[1,1,0] op_sel_hi:[1,0,1] neg_lo:[0,1,0]" : "=&v"(t) : "v"(x), "v"(y));
    return t;
}
#define DSK(k) (((k) & 2) ? bhi(Dsk[((k) >> 2) * 2 + ((k) & 1)]) : blo(Dsk[((k) >> 2) * 2 + ((k) & 1)]))
__device__ __forceinline__ v4u half_xpose(u32 c0a, u32 c0b, u32 c1a, u32 c1b) {
    auto sa = __builtin_amdgcn_permlane32_swap(c0a, c1a, false, false);
    auto sb = __builtin_amdgcn_permlane32_swap(c0b, c1b, false, false);
    return (v4u){sa[0], sb[0], sa[1], sb[1]};
}

__device__ __forceinline__ void get_y(rsrc_t ws, u32 off, u32 so0, u32 so1, u32 (&y)[8], bool& dead) {
    v4u y0, y1;
    poll2(ws, off, so0, so1, y0, y1, dead);
    wait_vm0();
    u32 o[4];
    to_rowlay(y0, o);
#pragma unroll
    for (int k = 0; k < 4; ++k) y[k] = o[k];
    to_rowlay(y1, o);
#pragma unroll
    for (int k = 0; k < 4; ++k) y[4 + k] = o[k];
}
__device__ __forceinline__ void put_u(rsrc_t ws, u32 off, u32 so, const float (&da)[8], const float (&db)[8], const Stat& s, float na, float nb) {
#pragma unroll
    for (int e = 0; e < 2; ++e) {
        u32 uh[4];
#pragma unroll
        for (int k = 0; k < 4; ++k) uh[k] = ns(pkbf(__builtin_fmaf(da[e * 4 + k], s.ra, na), __builtin_fmaf(db[e * 4 + k], s.rb, nb)));
        st16(ws, off, so + e * 262144u, from_rowlay(uh));
    }
}

__global__ void __launch_bounds__(512, 2) mega(Params p) {
    __shared__ __attribute__((aligned(16))) char smem[L_TOTAL];
    const int t = __builtin_amdgcn_workitem_id_x(), lane = t & 63;
    const int w = __builtin_amdgcn_readfirstlane(t >> 6);
    const int b = __builtin_amdgcn_workgroup_id_x();
    const int cid = (b & 7) * 32 + (b >> 3);
    const int half = b >> 7;
    const int rid = half * 128 + (b & 7) * 16 + ((b >> 3) & 15);
    const rsrc_t ws = mkr(p.ws, WS_BYTES);
    bool dead = false;
    const v4u sent4 = {SENT, SENT, SENT, SENT};
    const int li = lane & 15, lg = lane >> 4;
    const u32 sbase = (u32)(size_t)(LAS char*)smem;
    u64 pc0;
    asm volatile("s_getpc_b64 %0" : "=s"(pc0));
    STAMP(0);
    asm volatile("" :: "s"(p.q), "s"(p.k), "s"(p.v), "s"(p.sk), "s"(p.ssmk), "s"(p.ssmD), "s"(p.logA), "s"(p.Aim), "s"(p.Cr), "s"(p.Ci), "s"(p.logdt), "s"(p.Dskip), "s"(p.out));
    const u64 ep = *(const __attribute__((address_space(4))) u64*)(p.ws + WS_EPOCH);
    const int kch = (w >> 1) & 1, kn = (w & 1) * 64 + lane;
    char* scr = smem + L_SCR + kch * SCR_CH + kn * 4;
    const int slot = cid * 2 + kch;
    const int chan = (slot & ~63) + 2 * (slot & 31) + ((slot >> 5) & 1);
    const u32 o_w = (u32)lg * 65536u + (u32)li * 16u;
    const u32 s_w = WS_U + (u32)rid * 256u + (u32)(w & 3) * 524288u;
    const u32 o_yc = (u32)((lane >> 3) & 3) * 65536u + (u32)(lane >> 5) * 128u + (u32)(lane & 7) * 16u;
    const u32 s_yc = WS_Y + (u32)w * 262144u + (u32)cid * 256u;
    const u32 s_yw = WS_Y + (u32)(w & 1) * 131072u + (u32)slot * 128u;
#define TAG() u64 e1_ = ep; asm volatile("" : "+s"(e1_)); e1_ += 1ull; const v2u tag = {(u32)e1_, (u32)(e1_ >> 32)}
    if (w < 4) {
        const int ch = w >> 1, par = w & 1;
        const u32 pf = ldw(mkr((const void*)(pc0 & ~127ull), CODE_BYTES), t * 128, 0);
        st16(ws, o_w, s_w, sent4);
        st16(ws, o_w, s_w + 262144u, sent4);
        const KP kp = kg_load(p, chan, kn);
        u32 pcr, pci, pcr2, pci2;
        {
            const rsrc_t rcr = mkr(p.Cr, DM * NST * 2), rci = mkr(p.Ci, DM * NST * 2);
            pcr = ldh(rcr, kn * 2, chan * (NST * 2)); pci = ldh(rci, kn * 2, chan * (NST * 2));
            pcr2 = ldh(rcr, (kn ^ 64) * 2, chan * (NST * 2)); pci2 = ldh(rci, (kn ^ 64) * 2, chan * (NST * 2));
        }
        *(v4u*)(smem + L_U + w * U_CH + lane * 16) = (v4u){0u, 0u, 0u, 0u};
        {
            const u32 ia = w == 0 ? L_RR + lane * 16 : w == 1 ? L_CNT + li * 16 : w == 2 ? L_T + (li >> 3) * T_CH + ((li & 7) * 258 + 257) * 16 : L_KL + (lane & 1) * KL_CH;
            const u32 iv = w == 0 ? SENT : 0u;
            *(v4u*)(smem + ia) = (v4u){iv, iv, iv, iv};
        }
        bar();
        asm volatile("s_waitcnt vmcnt(7)" ::: "memory");
        lsig(sbase + C_K1);
        TAG();
        const u32 o_fs = lane == 0 ? WS_FLAG : 0x80000000u;
#define STFLAG(so) st8(ws, o_fs, so, tag)
        STFLAG((w * 256 + cid) * 8);
        const u32 pf1 = ldw(ws, (u32)lane * MB2 + (WS_U - 128u), 0);
        const u32 pf2 = ldw(mkr(p.out, SL * DM * 2), 0, rid * 8192);
        v4u sa0[2], sa1[2];
        auto late_loads = [&]() {
            FENCE();
        {
            const rsrc_t rs = mkr(p.ssmk + (size_t)chan * SL, SL * 2);
            const u32 o = (u32)(255 - (t & 127)) * 16u;
            sa1[0] = __builtin_amdgcn_raw_buffer_load_b128(rs, o, 0, 0);
            sa0[0] = __builtin_amdgcn_raw_buffer_load_b128(rs, o - 16u, 0, 0);
            sa1[1] = __builtin_amdgcn_raw_buffer_load_b128(rs, o - 2048u, 0, 0);
            sa0[1] = __builtin_amdgcn_raw_buffer_load_b128(rs, o - 2064u, 0, 0);
        }
        FENCE();
        };
        auto y0_clear = [&]() {
            FENCE();
            st16(ws, o_yc, s_yc, sent4);
            st16(ws, o_yc, s_yc + MB1, sent4);
            FENCE();
        };
        if (YC_POS == 0) y0_clear();
        if (SSMK_POS == 0) late_loads();
        SETPRIO(CPRIO);
        STAMP(7);
        {
            const KG g = kg_prep(kp);
            u32 cm = umax(umax(pcr & 0x7fffu, pci & 0x7fffu), umax(pcr2 & 0x7fffu, pci2 & 0x7fffu));
            cm = umax(cm, dppu<XOR1>(cm));
            cm = umax(cm, dppu<XOR2>(cm));
            cm = umax(cm, dppu<HMIR>(cm));
            cm = umax(cm, dppu<MIR>(cm));
            { auto sw = __builtin_amdgcn_permlane16_swap(cm, cm, false, false); cm = umax(sw[0], sw[1]); }
            { auto sw = __builtin_amdgcn_permlane32_swap(cm, cm, false, false); cm = umax(sw[0], sw[1]); }
            const int e = (int)(cm >> 7);
            const int sf = 267 - e < 253 ? 267 - e : 253;
            const float scale = u2f((u32)sf << 23);
            *(float*)(smem + L_YSC + kch * 4) = 2.f * g.dt * u2f((u32)(254 - sf) << 23);
            cf G[4], F[8];
            G[0] = {blo(pcr) * scale, blo(pci) * scale};
            const cf E7 = CEXP(7), E8 = CEXP(8), E9 = CEXP(9);
            G[1] = cmul(G[0], E7); G[2] = cmul(G[0], E8); G[3] = cmul(G[1], E8);
            F[1] = CEXP(0); F[2] = CEXP(1); F[4] = CEXP(2);
            F[3] = cmul(F[1], F[2]); F[5] = cmul(F[1], F[4]); F[6] = cmul(F[2], F[4]); F[7] = cmul(F[3], F[4]);
            u32 gp[4], fp[8];
#pragma unroll
            for (int a = 0; a < 4; ++a) gp[a] = pkc(G[a]);
#pragma unroll
            for (int a = 1; a < 8; ++a) fp[a] = pkc(F[a]);
            PUTP(0, gp[0]); PUTP(8, gp[1]); PUTP(16, gp[2]); PUTP(20, gp[3]);
            if (YC_POS == 1) y0_clear();
            if (SSMK_POS == 1) late_loads();
#pragma unroll
            for (int e2 = 1; e2 < 8; ++e2) { PUTP(e2, hmul(gp[0], fp[e2])); PUTP(8 + e2, hmul(gp[1], fp[e2])); }
#pragma unroll
            for (int e2 = 1; e2 < 4; ++e2) { PUTP(16 + e2, hmul(gp[2], fp[2 * e2])); PUTP(20 + e2, hmul(gp[3], fp[2 * e2])); }
            const u32 e9 = pkc(E9), e10 = pkc(CEXP(10));
#pragma unroll
            for (int a = 0; a < 4; ++a) {
                const u32 h = pkc(cmul(G[a], E9));
                PUTP(24 + 2 * a, h); PUTP(25 + 2 * a, hmul(h, fp[4])); PUTP(32 + a, hmul(h, e9)); PUTP(36 + a, hmul(h, e10));
            }
            PUTP(40, hmul(gp[0], pkc(CEXP(11))));
        }
        lsig(sbase + C_K);
        if (YC_POS == 2) y0_clear();
        STAMP(1);
        const u32 o_fl1 = WS_FLAG + (u32)((cid >> 6) * 256 + (t >> 5) * 32 + (t & 1) * 16 + ((t >> 1) & 15)) * 8u;
        v2u fl = __builtin_amdgcn_raw_buffer_load_b64(ws, o_fl1, 0, 16);
        asm volatile("" :: "v"(pf), "v"(pf1), "v"(pf2));
        char* tslot = smem + L_T + ch * T_CH + ((t & 127) + 1) * 16;
        build_T(tslot, sa0[0], sa1[0]);
        build_T(tslot + 2048, sa0[1], sa1[1]);
        lsig(sbase + C_CONV);
        const int rho = li & 7;
        const u32 tb = sbase + L_T + ch * T_CH + ((rho < 4 ? 2 * rho : 2 * (rho - 4) + 1) * 258 + 2 - (li >> 3) + lg) * 16;
        const u32 ub = sbase + L_U + ch * U_CH + (4 * (li & 8) + 16 * par + 2 * (li & 7) + lg + 2) * 16;
        const u32 o_yw = (u32)(lg & 1) * 524288u + (u32)(li & 8) * 32768u + (u32)(li & 4) * 16384u + (u32)(li & 3) * 32u + (u32)(lg & 2) * 8u;
        const u32 o_ur = (u32)(t >> 1) * 256u + (u32)(t & 1) * 16u;
        const u32 s_ur = WS_U + (u32)(cid >> 3) * 65536u + (u32)(cid & 7) * 32u;
        char* uslot = smem + L_U + (t & 1) * U_CH + (64 + (t >> 1)) * 16;
        STAMP(26);
        pollf(ws, o_fl1, 0, fl, tag, dead);
        lwait(smem + C_ROW, 4);
        STAMP(2);
        {
            v4u k0 = ld16(ws, o_ur, s_ur), k1 = ld16(ws, o_ur, s_ur + 32768u);
            STFLAG((w * 256 + cid) * 8 + 8192);
            lwait(smem + C_CONV, 4);
            asm volatile(TLOAD_ASM :: "v"(tb) : "memory", TLOAD_CLOB);
            pin(k0); pin(k1);
            if (anybad(k0, k1)) poll2(ws, o_ur, s_ur, s_ur + 32768u, k0, k1, dead);
            *(v4u*)uslot = k0;
            *(v4u*)(uslot + 2048) = k1;
        }
        STAMP(3);
        SETPRIO(1);
        v4f a0 = {0.f, 0.f, 0.f, 0.f}, a1 = a0, a2 = a0, a3 = a0;
        u32 seq = 4;
        u32 tq = 0;
#pragma unroll 1
        for (int h = 0; h < 6; ++h) {
            const int L = h >> 1, s = h & 1;
            const int uo = (L & 1) * 2 * U_CH + s * 2048;
            if (h >= 2) {
                if (s == 0) {
                    fl = __builtin_amdgcn_raw_buffer_load_b64(ws, o_fl1, 8192 + L * 8192, 16);
                    pollf(ws, o_fl1, 8192 + L * 8192, fl, tag, dead);
                    lwait(smem + C_RY, 2 * h);
                } else {
                    u32 n = 0;
                    while ((int)((u32)__builtin_amdgcn_s_memrealtime() - tq) < 0 && ++n < 1000) __builtin_amdgcn_s_sleep(1);
                }
                STAMP(3 + h * 4);
                const u32 so = s_ur + L * MB2 + s * 32768u;
                v4u uin;
                poll1(ws, o_ur, so, uin, dead);
                *(v4u*)(uslot + uo) = uin;
            }
            lsig(sbase + C_CONV);
            if ((h & 5) == 0) {
                st16(ws, o_w, s_w + (L + 1) * MB2, sent4);
                st16(ws, o_w, s_w + (L + 1) * MB2 + 262144u, sent4);
            }
            if (s) STFLAG((w * 256 + cid) * 8 + 16384 + L * 8192);
            seq += 4;
            STAMP(4 + h * 4);
            const u32 u = ub + uo;
            {
                u32 tg = seq * LINC + 1u, tmp;
                asm volatile(TRI_ASM : "+v"(a0), "+v"(a1), "+v"(a2), "+v"(a3), "+s"(tg), "=&s"(tmp) : "v"(sbase + C_CONV), "v"(u) : "memory", "scc", CONV_CLOB);
            }
            a0 += a1; a2 += a3;
            {
                auto s0 = __builtin_amdgcn_permlane16_swap(ns(pkbf(a0[0], a0[1])), ns(pkbf(a2[0], a2[1])), false, false);
                auto s1 = __builtin_amdgcn_permlane16_swap(ns(pkbf(a0[2], a0[3])), ns(pkbf(a2[2], a2[3])), false, false);
                st16(ws, o_yw, s_yw + L * MB2 + s * MB1, (v4u){s0[0], s1[0], s0[1], s1[1]});
            }
            lsig(sbase + C_Y);
            STAMP(5 + h * 4);
            a0 = (v4f){0.f, 0.f, 0.f, 0.f}; a1 = a0; a2 = a0; a3 = a0;
            if (s == 0) {
                if (h < 4) {
                    st16(ws, o_yc, s_yc + (L + 1) * MB2, sent4);
                    st16(ws, o_yc, s_yc + (L + 1) * MB2 + MB1, sent4);
                }
                asm volatile(CROSS_ASM : "+v"(a0), "+v"(a1), "+v"(a2), "+v"(a3) : "v"(u), "v"(tb) : "memory", CONV_CLOB);
                wait_vm0();
                lsig(sbase + C_CL);
                STAMP(6 + h * 4);
            } else {
                tq = (u32)__builtin_amdgcn_s_memrealtime() + (h < 2 ? DLY_U0 : DLY_U1);
                if (h == 1) {
                    lwait(smem + C_ROW, 12);
                    STAMP(6 + h * 4);
                    const char* kl = smem + L_KL + ch * KL_CH + (127 - (t & 127)) * 16;
                    build_T(tslot, *(const v4u*)(kl + 2048), *(const v4u*)(kl + 2064));
                    build_T(tslot + 2048, *(const v4u*)kl, *(const v4u*)(kl + 16));
                    lsig(sbase + C_CONV);
                    seq += 4;
                    lwait(smem + C_CONV, seq);
                    asm volatile(TLOAD_ASM :: "v"(tb) : "memory", TLOAD_CLOB);
                    STAMP(27);
                }
            }
        }
        STAMP_OUT();
        return;
    }
    const int r = w - 4;
    SETPRIO(3);
    const u32 o_rw = (u32)(lane >> 5) * 4096u + (u32)(lane & 31) * 4u;
    const u32 s_rw = (u32)rid * 8192u + (u32)r * 256u;
    u32 kk[2][7], vv[2][4];
    v4u sk4[2];
    const KP kp = kg_load(p, chan, kn);
    FENCE();
    {
        const rsrc_t rk = mkr(p.k, SL * DM * 2), rv = mkr(p.v, SL * DM * 2);
        const u32 ok = o_rw + s_rw - 3072u;
#pragma unroll
        for (int e = 0; e < 2; ++e) {
#pragma unroll
            for (int j = 0; j < 3; ++j) kk[e][j] = ldw(rk, ok + j * 1024u + e * 128u, 0);
#pragma unroll
            for (int j = 0; j < 4; ++j) kk[e][3 + j] = ldw(rk, o_rw + j * 1024u + e * 128u, s_rw);
#pragma unroll
            for (int j = 0; j < 4; ++j) vv[e][j] = ldw(rv, o_rw + j * 1024u + e * 128u, s_rw);
            sk4[e] = __builtin_amdgcn_raw_buffer_load_b128(mkr(p.sk, DM * 8), (lane & 31) * 16 + e * 512, r * 1024, 0);
        }
    }
    STAMP(6);
    bar();
    SETPRIO(RPRIO);
    {
        const KG g = kg_prep(kp);
        cf L[9];
        L[1] = CEXP(3); L[2] = CEXP(4); L[4] = CEXP(5); L[8] = CEXP(6);
        L[3] = cmul(L[1], L[2]); L[5] = cmul(L[1], L[4]); L[6] = cmul(L[2], L[4]); L[7] = cmul(L[3], L[4]);
        PUTP(41, 0x3c00u);
        const u32 l8 = pkp(L[8]);
#pragma unroll
        for (int i = 1; i < 8; ++i) { const u32 l = pkp(L[i]); PUTP(41 + i, l); PUTP(49 + i, hmul(l, l8)); }
        PUTP(49, l8);
    }
    asm volatile("" : "+v"(kk[0][0]), "+v"(kk[0][1]), "+v"(kk[0][2]), "+v"(kk[0][3]), "+v"(kk[0][4]), "+v"(kk[0][5]), "+v"(kk[0][6]), "+v"(sk4[0]) :: "memory");
    SETPRIO(3);
    STAMP(20);
    u32 qq[2][4], Dch[2], pil, Dsk[4];
    auto q_loads = [&]() {
    {
        const rsrc_t rq = mkr(p.q, SL * DM * 2);
#pragma unroll
        for (int e = 0; e < 2; ++e) {
#pragma unroll
            for (int j = 0; j < 4; ++j) qq[e][j] = ldw(rq, o_rw + j * 1024u + e * 128u, s_rw);
            Dch[e] = ldh(mkr(p.ssmD, DM * 2), (lane & 31) * 4 + (lane >> 5) * 2 + e * 128, r * 256);
        }
        pil = ldh(rq, lg * 2048, rid * 8192) | (ldh(rq, lg * 2048 + 1024, rid * 8192) << 16);
    }
    {
        const rsrc_t rd2 = mkr(p.Dskip, DM * 2);
#pragma unroll
        for (int k = 0; k < 4; ++k) Dsk[k] = ldw(rd2, li * 4 + k * 64, r * 256);
    }
        FENCE();
    };
    if (Q_POS == 0) q_loads();
    FENCE();
    v4u kvp[2];
    {
#pragma unroll
        for (int e = 0; e < 2; ++e) {
            const float w00 = blo(sk4[e].x), w01 = bhi(sk4[e].x), w02 = blo(sk4[e].y), w03 = bhi(sk4[e].y);
            const float w10 = blo(sk4[e].z), w11 = bhi(sk4[e].z), w12 = blo(sk4[e].w), w13 = bhi(sk4[e].w);
            float a0[4], a1[4];
#pragma unroll
            for (int j = 0; j < 4; ++j) {
                const float s0 = __builtin_fmaf(blo(kk[e][j + 3]), w00, __builtin_fmaf(blo(kk[e][j + 2]), w01, __builtin_fmaf(blo(kk[e][j + 1]), w02, blo(kk[e][j]) * w03)));
                const float s1 = __builtin_fmaf(bhi(kk[e][j + 3]), w10, __builtin_fmaf(bhi(kk[e][j + 2]), w11, __builtin_fmaf(bhi(kk[e][j + 1]), w12, bhi(kk[e][j]) * w13)));
                a0[j] = s0 * blo(vv[e][j]);
                a1[j] = s1 * bhi(vv[e][j]);
            }
            kvp[e] = half_xpose(ns(pkbf(a0[0], a0[1])), ns(pkbf(a0[2], a0[3])), ns(pkbf(a1[0], a1[1])), ns(pkbf(a1[2], a1[3])));
            if (e == 0) lwait(smem + C_K1, 4);
            st16(ws, o_w, s_w + e * 262144u, kvp[e]);
        }
    }
    STAMP(1);
    SETPRIO(0);
    lsig(sbase + C_ROW);
    if (Q_POS == 1) q_loads();
    int io = (r & 1) == 0 ? 128 * (lg >> 1) + 4 * (lg & 1) + 8 * li : 256 + 64 * lg + (lg < 2 ? 4 : 2) * li;
    int st2 = (r & 1) && lg >= 2 ? 32 : 2;
    pin(io); pin(st2);
    lwait(smem + C_K, 4);
    lwait(smem + C_ROW, 4);
    STAMP(2);
    {
        const char* bb = smem + L_SCR + kch * SCR_CH + (41 + li) * SCR_ROW + lg * 16;
        const char* ab = smem + L_SCR + kch * SCR_CH + ((r & 1) * 16 + li) * SCR_ROW + lg * 16;
        const float ysc = *(const float*)(smem + L_YSC + kch * 4);
        v4f c0 = {0.f, 0.f, 0.f, 0.f}, c1 = c0;
#pragma unroll
        for (int ks = 0; ks < 8; ++ks) {
            const v8h bf = *(const v8h*)(bb + ks * 64);
            c0 = __builtin_amdgcn_mfma_f32_16x16x32_f16(*(const v8h*)(ab + ks * 64), bf, c0, 0, 0, 0);
            c1 = __builtin_amdgcn_mfma_f32_16x16x32_f16(*(const v8h*)(bb - 9 * SCR_ROW + ks * 64), bf, c1, 0, 0, 0);
        }
        SGB(0x100, 10);
#pragma unroll
        for (int ks = 0; ks < 5; ++ks) { SGB(0x8, 2); SGB(0x100, 3); }
        SGB(0x8, 6);
        char* tab = smem + L_KTAB + kch * 1536;
        asm volatile("s_nop 7\n\ts_nop 2" : "+a"(c0), "+a"(c1));
        *(u32*)(tab + io * 2) = pkbf(c0[0] * ysc, c0[1] * ysc);
        *(u32*)(tab + (io + st2) * 2) = pkbf(c0[2] * ysc, c0[3] * ysc);
        const u32 p01 = pkbf(c1[0] * ysc, c1[1] * ysc), p23 = pkbf(c1[2] * ysc, c1[3] * ysc);
        u16* t2 = (u16*)tab + 512 + 64 * lg + li;
        t2[0] = (u16)p01; t2[16] = (u16)(p01 >> 16); t2[32] = (u16)p23; t2[48] = (u16)(p23 >> 16);
    }
    lsig(sbase + C_ROW);
    STAMP(3);
    lwait(smem + C_ROW, 8);
    {
        const int c = 255 - (t & 255);
        v4u kc[2];
        if (r < 2) {
#pragma unroll
            for (int e = 0; e < 2; ++e) {
                const char* tab = smem + L_KTAB + e * 1536;
                const u32 a = *(const u16*)(tab + (384 + c) * 2), bq = *(const u16*)(tab + (385 + c) * 2);
                const u32 aa = a | (a << 16), bb = bq | (bq << 16);
                kc[e] = (v4u){aa, aa, (c & 1) ? bb : (a | (bq << 16)), bb};
            }
        } else if (r == 2) {
#pragma unroll
            for (int e = 0; e < 2; ++e) {
                const char* tab = smem + L_KTAB + e * 1536;
                const u32 d0 = *(const u32*)(tab + (256 + 2 * c) * 2), d1 = *(const u32*)(tab + (258 + 2 * c) * 2);
                kc[e] = (v4u){__builtin_amdgcn_perm(d0, d0, 0x01000100u), d0, __builtin_amdgcn_perm(d0, d0, 0x03020302u), __builtin_amdgcn_perm(d1, d1, 0x01000100u)};
            }
        } else {
            const bool s1 = c >= 32;
#pragma unroll
            for (int e = 0; e < 2; ++e) {
                const char* a = smem + L_KTAB + e * 1536 + (s1 ? (128 + 4 * c) * 2 : 16 * c);
                const v2u d01 = *(const v2u*)a, d23 = *(const v2u*)(a + 8);
                kc[e].x = s1 ? __builtin_amdgcn_perm(d01.x, d01.x, 0x01000100u) : d01.x;
                kc[e].y = s1 ? __builtin_amdgcn_alignbit(d01.y, d01.x, 16) : d01.y;
                kc[e].z = s1 ? __builtin_amdgcn_perm(d01.y, d01.y, 0x01000100u) : d23.x;
                kc[e].w = s1 ? __builtin_amdgcn_alignbit(d23.x, d01.y, 16) : d23.y;
            }
        }
        *(v4u*)(smem + L_KL + (c + 1) * 16) = kc[0];
        *(v4u*)(smem + L_KL + KL_CH + (c + 1) * 16) = kc[1];
    }
    lsig(sbase + C_ROW);
    STAMP(4);
    const u32 o_fl = WS_FLAG + (u32)((cid >> 6) * 256 + (t & 255)) * 8u;
    u32 x[8], yd[8];
    v2u fl;
    Stat s;
    float ga[8], gb[8], ca[8], cb[8];
    {
#pragma unroll
        for (int e = 0; e < 2; ++e) {
            u32 o[4];
            to_rowlay(half_xpose(__builtin_amdgcn_perm(qq[e][1], qq[e][0], 0x05040100u), __builtin_amdgcn_perm(qq[e][3], qq[e][2], 0x05040100u),
                                 __builtin_amdgcn_perm(qq[e][1], qq[e][0], 0x07060302u), __builtin_amdgcn_perm(qq[e][3], qq[e][2], 0x07060302u)), o);
#pragma unroll
            for (int k = 0; k < 4; ++k) x[e * 4 + k] = o[k];
        }
        s.ma = blo(pil); s.mb = bhi(pil);
        float da[8], db[8], na, nb;
#pragma unroll
        for (int k = 0; k < 8; ++k) { da[k] = blo(x[k]) - s.ma; db[k] = bhi(x[k]) - s.mb; }
        row_stats(smem + L_RR, r, lane, da, db, s, na, nb);
        fl = __builtin_amdgcn_raw_buffer_load_b64(ws, o_fl, 8192, 16);
        FENCE();
#pragma unroll
        for (int e = 0; e < 2; ++e) {
            const float d0 = blo(Dch[e]);
            u32 o[4];
            to_rowlay((v4u){pkbf(blo(kvp[e].x) * d0, bhi(kvp[e].x) * d0), pkbf(blo(kvp[e].y) * d0, bhi(kvp[e].y) * d0),
                            pkbf(blo(kvp[e].z) * d0, bhi(kvp[e].z) * d0), pkbf(blo(kvp[e].w) * d0, bhi(kvp[e].w) * d0)}, o);
#pragma unroll
            for (int k = 0; k < 4; ++k) yd[e * 4 + k] = o[k];
        }
#pragma unroll
        for (int k = 0; k < 8; ++k) {
            const float ea = blo(x[k]) - s.ma, eb = bhi(x[k]) - s.mb;
            const u32 pd = pkbf(ea, eb);
            const u32 n0 = pkbf(blo(pd) * s.ra, bhi(pd) * s.rb);
            ga[k] = blo(n0); gb[k] = bhi(n0);
            ca[k] = __builtin_fmaf(ga[k], blo(yd[k]), ea);
            cb[k] = __builtin_fmaf(gb[k], bhi(yd[k]), eb);
            pin(ca[k]); pin(cb[k]);
        }
    }
    TAG();
    pollf(ws, o_fl, 8192, fl, tag, dead);
    STAMP(5);
    SETPRIO(3);
    const u32 o_yr = (u32)lane * 128u;
    const u32 s_yr = WS_Y + (u32)(rid >> 3) * 65536u + (u32)(rid & 7) * 16u + (u32)r * 16384u;
    u32 y[8];
    float da[8], db[8], na, nb;
    lwait(smem + C_Y, 4 * half + 4);
    if (DLY_Y) __builtin_amdgcn_s_sleep(DLY_Y);
    STAMP(8);
    get_y(ws, o_yr, s_yr, s_yr + 8192u, y, dead);
    const u32 c_m1 = sbase + (half ? C_RY : C_DUM), c_m2 = sbase + (half ? C_DUM : C_RY);
    lsig(c_m1);
    lwait(smem + C_CL, 4);
    STAMP(9);
#pragma unroll
    for (int k = 0; k < 8; ++k) {
        da[k] = __builtin_fmaf(ga[k], blo(y[k]), ca[k]);
        db[k] = __builtin_fmaf(gb[k], bhi(y[k]), cb[k]);
    }
    row_stats(smem + L_RR + 256, r, lane, da, db, s, na, nb);
    put_u(ws, o_w, s_w + MB2, da, db, s, na, nb);
    STAMP(11);
    SETPRIO(0);
#pragma unroll
    for (int k = 0; k < 8; ++k) {
        const u32 z = add2(y[k], yd[k]);
        x[k] = add2(x[k], pkbf(ga[k] * blo(z), gb[k] * bhi(z)));
        ga[k] = blo(z); gb[k] = bhi(z);
        const float ea = blo(x[k]) - s.ma, eb = bhi(x[k]) - s.mb;
        const u32 pd = pkbf(ea, eb);
        const u32 u = pkbf(blo(pd) * s.ra, bhi(pd) * s.rb);
        const float dk = DSK(k);
        yd[k] = pkbf(blo(u) * dk, bhi(u) * dk);
        ca[k] = ea + blo(yd[k]);
        cb[k] = eb + bhi(yd[k]);
        pin(ca[k]); pin(cb[k]); pin(ga[k]); pin(gb[k]);
    }
    wait_vm0();
    lsig(c_m2);
    if (b == 0 && t == 256) st8(ws, WS_EPOCH, 0, tag);
    SETPRIO(3);
    STAMP(12);
    lwait(smem + C_Y, 4 * half + 12);
    if (DLY_Y) __builtin_amdgcn_s_sleep(DLY_Y);
    STAMP(15);
    get_y(ws, o_yr, s_yr + MB2, s_yr + MB2 + 8192u, y, dead);
    lsig(c_m1);
    lwait(smem + C_CL, 8);
    STAMP(16);
#pragma unroll
    for (int k = 0; k < 8; ++k) { da[k] = blo(y[k]) + ca[k]; db[k] = bhi(y[k]) + cb[k]; }
    Stat s2 = s;
    row_stats(smem + L_RR + 512, r, lane, da, db, s2, na, nb);
#pragma unroll
    for (int k = 0; k < 8; ++k) {
        da[k] = __builtin_fmaf(__builtin_fmaf(da[k], s2.ra, na), ga[k], da[k]);
        db[k] = __builtin_fmaf(__builtin_fmaf(db[k], s2.rb, nb), gb[k], db[k]);
    }
    STAMP(17);
    row_stats(smem + L_RR + 768, r, lane, da, db, s, na, nb);
    put_u(ws, o_w, s_w + 2 * MB2, da, db, s, na, nb);
    STAMP(18);
    SETPRIO(0);
    {
        char* tr = smem + L_SCR + r * 4352;
#pragma unroll
        for (int k = 0; k < 8; ++k) {
            u32 xx = add2(x[k], add2(y[k], yd[k]));
            const u32 n = ln2(xx, s2);
            xx = add2(xx, pkbf(blo(n) * ga[k], bhi(n) * gb[k]));
            const u32 u = ln2(xx, s);
            const float dk = DSK(k);
            const u32 d = pkbf(blo(u) * dk, bhi(u) * dk);
            const int co = (64 * (k >> 2) + 32 * (k & 1) + ((k >> 1) & 1)) * 4;
            *(float*)(tr + lg * 1088 + li * 8 + co) = blo(xx) + blo(d);
            *(float*)(tr + lg * 1088 + 544 + li * 8 + co) = bhi(xx) + bhi(d);
        }
        wait_vm0();
        lsig(c_m2);
#pragma unroll
        for (int e = 0; e < 2; ++e) {
            const v4f c0 = *(const v4f*)(tr + lg * 1088 + li * 32 + e * 16), c1 = *(const v4f*)(tr + lg * 1088 + 544 + li * 32 + e * 16);
#pragma unroll
            for (int k = 0; k < 4; ++k) { ca[e * 4 + k] = c0[k]; cb[e * 4 + k] = c1[k]; pin(ca[e * 4 + k]); pin(cb[e * 4 + k]); }
        }
    }
    SETPRIO(3);
    STAMP(19);
    lwait(smem + C_Y, 4 * half + 20);
    if (DLY_Y) __builtin_amdgcn_s_sleep(DLY_Y);
    STAMP(22);
    get_y(ws, (u32)(li & 8) * 1024u + (u32)(li & 7) * 512u + (u32)(lg >> 1) * 128u + (u32)(lg & 1) * 4096u, s_yr + 2 * MB2, s_yr + 2 * MB2 + 256u, y, dead);
    STAMP(23);
    {
        const rsrc_t ro = mkr(p.out, SL * DM * 2);
        const u32 oo = (u32)lg * 2048u + (u32)li * 16u;
        v4u o0, o1;
#pragma unroll
        for (int k = 0; k < 4; ++k) {
            o0[k] = pkbf(blo(y[2 * k]) + ca[2 * k], blo(y[2 * k + 1]) + ca[2 * k + 1]);
            o1[k] = pkbf(bhi(y[2 * k]) + cb[2 * k], bhi(y[2 * k + 1]) + cb[2 * k + 1]);
        }
        st16(ro, oo, s_rw, o0);
        st16(ro, oo + 1024u, s_rw, o1);
    }
    STAMP(28);
}
#if defined(__HIP_DEVICE_COMPILE__)
#pragma clang attribute pop
#endif

extern "C" void kernel_launch(void* const* d_in, const int* in_sizes, int n_in,
                              void* d_out, int out_size, void* d_ws, size_t ws_size,
                              hipStream_t stream) {
    Params p;
    p.q = (const u16*)d_in[0];
    p.k = (const u16*)d_in[1];
    p.v = (const u16*)d_in[2];
    p.sk = (const u16*)d_in[3];
    p.ssmk = (const u16*)d_in[4];
    p.ssmD = (const u16*)d_in[5];
    p.logA = (const u16*)d_in[7];
    p.Aim = (const u16*)d_in[8];
    p.Cr = (const u16*)d_in[9];
    p.Ci = (const u16*)d_in[10];
    p.logdt = (const u16*)d_in[11];
    p.Dskip = (const u16*)d_in[12];
    p.out = (u16*)d_out;
    p.ws = (char*)d_ws;
    mega<<<256, 512, 0, stream>>>(p);
}
```

```cpp
#if defined(__HIP_DEVICE_COMPILE__)
#pragma clang attribute push(__attribute__((target("no-packed-fp32-ops"))), apply_to = function)
#endif
#include <hip/hip_runtime.h>
#include <stdint.h>

typedef unsigned short u16;
typedef unsigned u32;
typedef unsigned long long u64;
typedef u32 v4u __attribute__((ext_vector_type(4)));
typedef u32 v2u __attribute__((ext_vector_type(2)));
typedef float v4f __attribute__((ext_vector_type(4)));
typedef float v2f __attribute__((ext_vector_type(2)));
typedef __bf16 v2bf __attribute__((ext_vector_type(2)));
typedef __bf16 v8bf __attribute__((ext_vector_type(8)));
typedef _Float16 v8h __attribute__((ext_vector_type(8)));
typedef _Float16 v2h __attribute__((ext_vector_type(2)));
#define GAS __attribute__((address_space(1)))
#define LAS __attribute__((address_space(3)))
typedef GAS u64 gu64;
#define RLX_AGENT __ATOMIC_RELAXED, __HIP_MEMORY_SCOPE_AGENT

constexpr int SL = 2048, DM = 512, NST = 128;
constexpr u32 SENT = 0xFFFFFFFFu;
constexpr u32 SPIN_CAP = 1u << 15;

constexpr u32 WS_EPOCH = 0;
constexpr u32 WS_FLAG = 4096;
constexpr u32 MB1 = 1u << 20;
constexpr u32 MB2 = (2u << 20) + 65536u;
constexpr u32 WS_U = 65536u;
constexpr u32 WS_Y = WS_U + 3u * MB2;
constexpr u32 WS_BYTES = WS_U + 6u * MB2;
constexpr u32 LSPIN_CAP = 1u << 20;

constexpr int L_RR = 0;
constexpr int L_CNT = 1024;
constexpr int C_CONV = L_CNT, C_ROW = L_CNT + 64, C_Y = L_CNT + 128, C_K = L_CNT + 192, C_K1 = L_CNT + 224, L_YSC = L_CNT + 240, C_RY = L_CNT + 96, C_DUM = L_CNT + 160, C_CL = L_CNT + 32;
constexpr u32 LINC = 1u << 17;
constexpr int T_CH = 8 * 258 * 16;
constexpr int L_T = 1280;
constexpr int SCR_ROW = 528;
constexpr int SCR_CH = 57 * SCR_ROW;
constexpr int L_SCR = L_T + 2 * T_CH;
constexpr int U_CH = 320 * 16;
constexpr int L_U = L_SCR + 2 * SCR_CH;
constexpr int L_KTAB = L_U + 4 * U_CH;
constexpr int L_KL = L_KTAB + 2 * 1536;
constexpr int KL_CH = 257 * 16;
constexpr int L_TOTAL0 = L_KL + 2 * KL_CH;

constexpr int L_TOTAL = L_TOTAL0;
#define STAMP(i) do {} while (0)
#define STAMP_OUT() do {} while (0)
static_assert(L_TOTAL <= 163840, "lds");
struct Params {
    const u16 *q, *k, *v, *sk, *ssmk, *ssmD, *logA, *Aim, *Cr, *Ci, *logdt, *Dskip;
    u16* out;
    char* ws;
};

#define TLOAD_ASM \
    "ds_read_b128 a[0:3], %0 offset:4032\n\t" \
    "ds_read_b128 a[32:35], %0 offset:3520\n\t" \
    "ds_read_b128 a[64:67], %0 offset:3008\n\t" \
    "ds_read_b128 a[96:99], %0 offset:2496\n\t" \
    "ds_read_b128 a[4:7], %0 offset:3968\n\t" \
    "ds_read_b128 a[36:39], %0 offset:3456\n\t" \
    "ds_read_b128 a[68:71], %0 offset:2944\n\t" \
    "ds_read_b128 a[100:103], %0 offset:2432\n\t" \
    "ds_read_b128 a[8:11], %0 offset:3904\n\t" \
    "ds_read_b128 a[40:43], %0 offset:3392\n\t" \
    "ds_read_b128 a[72:75], %0 offset:2880\n\t" \
    "ds_read_b128 a[104:107], %0 offset:2368\n\t" \
    "ds_read_b128 a[12:15], %0 offset:3840\n\t" \
    "ds_read_b128 a[44:47], %0 offset:3328\n\t" \
    "ds_read_b128 a[76:79], %0 offset:2816\n\t" \
    "ds_read_b128 a[108:111], %0 offset:2304\n\t" \
    "ds_read_b128 a[16:19], %0 offset:3776\n\t" \
    "ds_read_b128 a[48:51], %0 offset:3264\n\t" \
    "ds_read_b128 a[80:83], %0 offset:2752\n\t" \
    "ds_read_b128 a[112:115], %0 offset:2240\n\t" \
    "ds_read_b128 a[20:23], %0 offset:3712\n\t" \
    "ds_read_b128 a[52:55], %0 offset:3200\n\t" \
    "ds_read_b128 a[84:87], %0 offset:2688\n\t" \
    "ds_read_b128 a[116:119], %0 offset:2176\n\t" \
    "ds_read_b128 a[24:27], %0 offset:3648\n\t" \
    "ds_read_b128 a[56:59], %0 offset:3136\n\t" \
    "ds_read_b128 a[88:91], %0 offset:2624\n\t" \
    "ds_read_b128 a[120:123], %0 offset:2112\n\t" \
    "ds_read_b128 a[28:31], %0 offset:3584\n\t" \
    "ds_read_b128 a[60:63], %0 offset:3072\n\t" \
    "ds_read_b128 a[92:95], %0 offset:2560\n\t" \
    "ds_read_b128 a[124:127], %0 offset:2048\n\t"
#define TLOAD_CLOB "a0", "a1", "a2", "a3", "a4", "a5", "a6", "a7", "a8", "a9", "a10", "a11", "a12", "a13", "a14", "a15", "a16", "a17", "a18", "a19", "a20", "a21", "a22", "a23", "a24", "a25", "a26", "a27", "a28", "a29", "a30", "a31", "a32", "a33", "a34", "a35", "a36", "a37", "a38", "a39", "a40", "a41", "a42", "a43", "a44", "a45", "a46", "a47", "a48", "a49", "a50", "a51", "a52", "a53", "a54", "a55", "a56", "a57", "a58", "a59", "a60", "a61", "a62", "a63", "a64", "a65", "a66", "a67", "a68", "a69", "a70", "a71", "a72", "a73", "a74", "a75", "a76", "a77", "a78", "a79", "a80", "a81", "a82", "a83", "a84", "a85", "a86", "a87", "a88", "a89", "a90", "a91", "a92", "a93", "a94", "a95", "a96", "a97", "a98", "a99", "a100", "a101", "a102", "a103", "a104", "a105", "a106", "a107", "a108", "a109", "a110", "a111", "a112", "a113", "a114", "a115", "a116", "a117", "a118", "a119", "a120", "a121", "a122", "a123", "a124", "a125", "a126", "a127"
#define CONV_CLOB "v64", "v65", "v66", "v67", "v68", "v69", "v70", "v71", "v72", "v73", "v74", "v75", "v76", "v77", "v78", "v79", "v80", "v81", "v82", "v83", "v84", "v85", "v86", "v87", "v88", "v89", "v90", "v91", "v92", "v93", "v94", "v95", "v96", "v97", "v98", "v99", "v100", "v101", "v102", "v103", "v104", "v105", "v106", "v107", "v108", "v109", "v110", "v111", "v112", "v113", "v114", "v115", "v116", "v117", "v118", "v119", "v120", "v121", "v122", "v123", "v124", "v125", "v126", "v127"
#define TRI_ASM \
    "1:\n\t" \
    "s_sub_u32 %4, %4, 1\n\t" \
    "ds_read_b32 v112, %6\n\t" \
    "ds_read_b128 v[64:67], %7 offset:1984\n\t" \
    "ds_read_b128 v[68:71], %7 offset:1472\n\t" \
    "ds_read_b128 v[72:75], %7 offset:960\n\t" \
    "ds_read_b128 v[76:79], %7 offset:448\n\t" \
    "ds_read_b128 v[80:83], %7 offset:1920\n\t" \
    "ds_read_b128 v[84:87], %7 offset:1408\n\t" \
    "ds_read_b128 v[88:91], %7 offset:896\n\t" \
    "ds_read_b128 v[92:95], %7 offset:384\n\t" \
    "ds_read_b128 v[96:99], %7 offset:1856\n\t" \
    "ds_read_b128 v[100:103], %7 offset:1344\n\t" \
    "ds_read_b128 v[104:107], %7 offset:832\n\t" \
    "ds_read_b128 v[108:111], %7 offset:320\n\t" \
    "s_waitcnt lgkmcnt(12)\n\t" \
    "v_readfirstlane_b32 %5, v112\n\t" \
    "s_cmp_lt_u32 %5, %4\n\t" \
    "s_cbranch_scc1 1b\n\t" \
    "s_waitcnt lgkmcnt(11)\n\t" \
    "v_mfma_f32_16x16x32_bf16 %2, a[0:3], v[64:67], %2\n\t" \
    "ds_read_b128 v[112:115], %7 offset:1792\n\t" \
    "s_waitcnt lgkmcnt(11)\n\t" \
    "v_mfma_f32_16x16x32_bf16 %3, a[32:35], v[68:71], %3\n\t" \
    "ds_read_b128 v[116:119], %7 offset:1280\n\t" \
    "s_waitcnt lgkmcnt(11)\n\t" \
    "v_mfma_f32_16x16x32_bf16 %0, a[0:3], v[72:75], %0\n\t" \
    "ds_read_b128 v[120:123], %7 offset:768\n\t" \
    "v_mfma_f32_16x16x32_bf16 %2, a[64:67], v[72:75], %2\n\t" \
    "ds_read_b128 v[124:127], %7 offset:256\n\t" \
    "s_waitcnt lgkmcnt(12)\n\t" \
    "v_mfma_f32_16x16x32_bf16 %1, a[32:35], v[76:79], %1\n\t" \
    "v_mfma_f32_16x16x32_bf16 %3, a[96:99], v[76:79], %3\n\t" \
    "s_waitcnt lgkmcnt(10)\n\t" \
    "v_mfma_f32_16x16x32_bf16 %2, a[4:7], v[80:83], %2\n\t" \
    "ds_read_b128 v[64:67], %7 offset:1728\n\t" \
    "v_mfma_f32_16x16x32_bf16 %3, a[36:39], v[84:87], %3\n\t" \
    "ds_read_b128 v[68:71], %7 offset:1216\n\t" \
    "s_waitcnt lgkmcnt(10)\n\t" \
    "v_mfma_f32_16x16x32_bf16 %0, a[4:7], v[88:91], %0\n\t" \
    "ds_read_b128 v[72:75], %7 offset:704\n\t" \
    "v_mfma_f32_16x16x32_bf16 %2, a[68:71], v[88:91], %2\n\t" \
    "ds_read_b128 v[76:79], %7 offset:192\n\t" \
    "v_mfma_f32_16x16x32_bf16 %1, a[36:39], v[92:95], %1\n\t" \
    "v_mfma_f32_16x16x32_bf16 %3, a[100:103], v[92:95], %3\n\t" \
    "s_waitcnt lgkmcnt(10)\n\t" \
    "v_mfma_f32_16x16x32_bf16 %2, a[8:11], v[96:99], %2\n\t" \
    "ds_read_b128 v[80:83], %7 offset:1664\n\t" \
    "v_mfma_f32_16x16x32_bf16 %3, a[40:43], v[100:103], %3\n\t" \
    "ds_read_b128 v[84:87], %7 offset:1152\n\t" \
    "s_waitcnt lgkmcnt(10)\n\t" \
    "v_mfma_f32_16x16x32_bf16 %0, a[8:11], v[104:107], %0\n\t" \
    "ds_read_b128 v[88:91], %7 offset:640\n\t" \
    "v_mfma_f32_16x16x32_bf16 %2, a[72:75], v[104:107], %2\n\t" \
    "ds_read_b128 v[92:95], %7 offset:128\n\t" \
    "v_mfma_f32_16x16x32_bf16 %1, a[40:43], v[108:111], %1\n\t" \
    "v_mfma_f32_16x16x32_bf16 %3, a[104:107], v[108:111], %3\n\t" \
    "s_waitcnt lgkmcnt(10)\n\t" \
    "v_mfma_f32_16x16x32_bf16 %2, a[12:15], v[112:115], %2\n\t" \
    "ds_read_b128 v[96:99], %7 offset:1600\n\t" \
    "v_mfma_f32_16x16x32_bf16 %3, a[44:47], v[116:119], %3\n\t" \
    "ds_read_b128 v[100:103], %7 offset:1088\n\t" \
    "s_waitcnt lgkmcnt(10)\n\t" \
    "v_mfma_f32_16x16x32_bf16 %0, a[12:15], v[120:123], %0\n\t" \
    "ds_read_b128 v[104:107], %7 offset:576\n\t" \
    "v_mfma_f32_16x16x32_bf16 %2, a[76:79], v[120:123], %2\n\t" \
    "ds_read_b128 v[108:111], %7 offset:64\n\t" \
    "v_mfma_f32_16x16x32_bf16 %1, a[44:47], v[124:127], %1\n\t" \
    "v_mfma_f32_16x16x32_bf16 %3, a[108:111], v[124:127], %3\n\t" \
    "s_waitcnt lgkmcnt(10)\n\t" \
    "v_mfma_f32_16x16x32_bf16 %2, a[16:19], v[64:67], %2\n\t" \
    "ds_read_b128 v[112:115], %7 offset:1536\n\t" \
    "v_mfma_f32_16x16x32_bf16 %3, a[48:51], v[68:71], %3\n\t" \
    "ds_read_b128 v[116:119], %7 offset:1024\n\t" \
    "s_waitcnt lgkmcnt(10)\n\t" \
    "v_mfma_f32_16x16x32_bf16 %0, a[16:19], v[72:75], %0\n\t" \
    "ds_read_b128 v[120:123], %7 offset:512\n\t" \
    "v_mfma_f32_16x16x32_bf16 %2, a[80:83], v[72:75], %2\n\t" \
    "ds_read_b128 v[124:127], %7 offset:0\n\t" \
    "v_mfma_f32_16x16x32_bf16 %1, a[48:51], v[76:79], %1\n\t" \
    "v_mfma_f32_16x16x32_bf16 %3, a[112:115], v[76:79], %3\n\t" \
    "s_waitcnt lgkmcnt(10)\n\t" \
    "v_mfma_f32_16x16x32_bf16 %2, a[20:23], v[80:83], %2\n\t" \
    "v_mfma_f32_16x16x32_bf16 %3, a[52:55], v[84:87], %3\n\t" \
    "s_waitcnt lgkmcnt(8)\n\t" \
    "v_mfma_f32_16x16x32_bf16 %0, a[20:23], v[88:91], %0\n\t" \
    "v_mfma_f32_16x16x32_bf16 %2, a[84:87], v[88:91], %2\n\t" \
    "v_mfma_f32_16x16x32_bf16 %1, a[52:55], v[92:95], %1\n\t" \
    "v_mfma_f32_16x16x32_bf16 %3, a[116:119], v[92:95], %3\n\t" \
    "s_waitcnt lgkmcnt(6)\n\t" \
    "v_mfma_f32_16x16x32_bf16 %2, a[24:27], v[96:99], %2\n\t" \
    "v_mfma_f32_16x16x32_bf16 %3, a[56:59], v[100:103], %3\n\t" \
    "s_waitcnt lgkmcnt(4)\n\t" \
    "v_mfma_f32_16x16x32_bf16 %0, a[24:27], v[104:107], %0\n\t" \
    "v_mfma_f32_16x16x32_bf16 %2, a[88:91], v[104:107], %2\n\t" \
    "v_mfma_f32_16x16x32_bf16 %1, a[56:59], v[108:111], %1\n\t" \
    "v_mfma_f32_16x16x32_bf16 %3, a[120:123], v[108:111], %3\n\t" \
    "s_waitcnt lgkmcnt(2)\n\t" \
    "v_mfma_f32_16x16x32_bf16 %2, a[28:31], v[112:115], %2\n\t" \
    "v_mfma_f32_16x16x32_bf16 %3, a[60:63], v[116:119], %3\n\t" \
    "s_waitcnt lgkmcnt(0)\n\t" \
    "v_mfma_f32_16x16x32_bf16 %0, a[28:31], v[120:123], %0\n\t" \
    "v_mfma_f32_16x16x32_bf16 %2, a[92:95], v[120:123], %2\n\t" \
    "v_mfma_f32_16x16x32_bf16 %1, a[60:63], v[124:127], %1\n\t" \
    "v_mfma_f32_16x16x32_bf16 %3, a[124:127], v[124:127], %3\n\t" \
    "s_nop 7\n\t" \
    "s_nop 1\n\t"
#define CROSS_ASM \
    "ds_read_b128 v[64:67], %5 offset:1984\n\t" \
    "ds_read_b128 v[68:71], %5 offset:1472\n\t" \
    "ds_read_b128 v[72:75], %6 offset:1984\n\t" \
    "ds_read_b128 v[76:79], %6 offset:1472\n\t" \
    "ds_read_b128 v[80:83], %5 offset:960\n\t" \
    "ds_read_b128 v[84:87], %5 offset:448\n\t" \
    "ds_read_b128 v[88:91], %6 offset:960\n\t" \
    "ds_read_b128 v[92:95], %6 offset:448\n\t" \
    "ds_read_b128 v[96:99], %5 offset:1920\n\t" \
    "ds_read_b128 v[100:103], %5 offset:1408\n\t" \
    "ds_read_b128 v[104:107], %6 offset:1920\n\t" \
    "ds_read_b128 v[108:111], %6 offset:1408\n\t" \
    "ds_read_b128 v[112:115], %5 offset:896\n\t" \
    "s_waitcnt lgkmcnt(11)\n\t" \
    "v_mfma_f32_16x16x32_bf16 %0, a[64:67], v[64:67], %0\n\t" \
    "ds_read_b128 v[116:119], %5 offset:384\n\t" \
    "v_mfma_f32_16x16x32_bf16 %1, a[96:99], v[68:71], %1\n\t" \
    "ds_read_b128 v[120:123], %6 offset:896\n\t" \
    "s_waitcnt lgkmcnt(11)\n\t" \
    "v_mfma_f32_16x16x32_bf16 %2, v[72:75], v[64:67], %2\n\t" \
    "ds_read_b128 v[124:127], %6 offset:384\n\t" \
    "v_mfma_f32_16x16x32_bf16 %3, v[76:79], v[68:71], %3\n\t" \
    "ds_read_b128 v[64:67], %5 offset:1856\n\t" \
    "s_waitcnt lgkmcnt(11)\n\t" \
    "v_mfma_f32_16x16x32_bf16 %0, v[72:75], v[80:83], %0\n\t" \
    "ds_read_b128 v[68:71], %5 offset:1344\n\t" \
    "v_mfma_f32_16x16x32_bf16 %1, v[76:79], v[84:87], %1\n\t" \
    "ds_read_b128 v[72:75], %6 offset:1856\n\t" \
    "s_waitcnt lgkmcnt(11)\n\t" \
    "v_mfma_f32_16x16x32_bf16 %2, v[88:91], v[80:83], %2\n\t" \
    "ds_read_b128 v[76:79], %6 offset:1344\n\t" \
    "v_mfma_f32_16x16x32_bf16 %3, v[92:95], v[84:87], %3\n\t" \
    "ds_read_b128 v[80:83], %5 offset:832\n\t" \
    "s_waitcnt lgkmcnt(11)\n\t" \
    "v_mfma_f32_16x16x32_bf16 %0, a[68:71], v[96:99], %0\n\t" \
    "ds_read_b128 v[84:87], %5 offset:320\n\t" \
    "v_mfma_f32_16x16x32_bf16 %1, a[100:103], v[100:103], %1\n\t" \
    "ds_read_b128 v[88:91], %6 offset:832\n\t" \
    "s_waitcnt lgkmcnt(11)\n\t" \
    "v_mfma_f32_16x16x32_bf16 %2, v[104:107], v[96:99], %2\n\t" \
    "ds_read_b128 v[92:95], %6 offset:320\n\t" \
    "v_mfma_f32_16x16x32_bf16 %3, v[108:111], v[100:103], %3\n\t" \
    "ds_read_b128 v[96:99], %5 offset:1792\n\t" \
    "s_waitcnt lgkmcnt(11)\n\t" \
    "v_mfma_f32_16x16x32_bf16 %0, v[104:107], v[112:115], %0\n\t" \
    "ds_read_b128 v[100:103], %5 offset:1280\n\t" \
    "v_mfma_f32_16x16x32_bf16 %1, v[108:111], v[116:119], %1\n\t" \
    "ds_read_b128 v[104:107], %6 offset:1792\n\t" \
    "s_waitcnt lgkmcnt(11)\n\t" \
    "v_mfma_f32_16x16x32_bf16 %2, v[120:123], v[112:115], %2\n\t" \
    "ds_read_b128 v[108:111], %6 offset:1280\n\t" \
    "v_mfma_f32_16x16x32_bf16 %3, v[124:127], v[116:119], %3\n\t" \
    "ds_read_b128 v[112:115], %5 offset:768\n\t" \
    "s_waitcnt lgkmcnt(11)\n\t" \
    "v_mfma_f32_16x16x32_bf16 %0, a[72:75], v[64:67], %0\n\t" \
    "ds_read_b128 v[116:119], %5 offset:256\n\t" \
    "v_mfma_f32_16x16x32_bf16 %1, a[104:107], v[68:71], %1\n\t" \
    "ds_read_b128 v[120:123], %6 offset:768\n\t" \
    "s_waitcnt lgkmcnt(11)\n\t" \
    "v_mfma_f32_16x16x32_bf16 %2, v[72:75], v[64:67], %2\n\t" \
    "ds_read_b128 v[124:127], %6 offset:256\n\t" \
    "v_mfma_f32_16x16x32_bf16 %3, v[76:79], v[68:71], %3\n\t" \
    "ds_read_b128 v[64:67], %5 offset:1728\n\t" \
    "s_waitcnt lgkmcnt(11)\n\t" \
    "v_mfma_f32_16x16x32_bf16 %0, v[72:75], v[80:83], %0\n\t" \
    "buffer_load_dwordx4 %4, %7, %8, %9 offen sc1\n\t" \
    "ds_read_b128 v[68:71], %5 offset:1216\n\t" \
    "v_mfma_f32_16x16x32_bf16 %1, v[76:79], v[84:87], %1\n\t" \
    "ds_read_b128 v[72:75], %6 offset:1728\n\t" \
    "s_waitcnt lgkmcnt(11)\n\t" \
    "v_mfma_f32_16x16x32_bf16 %2, v[88:91], v[80:83], %2\n\t" \
    "ds_read_b128 v[76:79], %6 offset:1216\n\t" \
    "v_mfma_f32_16x16x32_bf16 %3, v[92:95], v[84:87], %3\n\t" \
    "ds_read_b128 v[80:83], %5 offset:704\n\t" \
    "s_waitcnt lgkmcnt(11)\n\t" \
    "v_mfma_f32_16x16x32_bf16 %0, a[76:79], v[96:99], %0\n\t" \
    "ds_read_b128 v[84:87], %5 offset:192\n\t" \
    "v_mfma_f32_16x16x32_bf16 %1, a[108:111], v[100:103], %1\n\t" \
    "ds_read_b128 v[88:91], %6 offset:704\n\t" \
    "s_waitcnt lgkmcnt(11)\n\t" \
    "v_mfma_f32_16x16x32_bf16 %2, v[104:107], v[96:99], %2\n\t" \
    "ds_read_b128 v[92:95], %6 offset:192\n\t" \
    "v_mfma_f32_16x16x32_bf16 %3, v[108:111], v[100:103], %3\n\t" \
    "ds_read_b128 v[96:99], %5 offset:1664\n\t" \
    "s_waitcnt lgkmcnt(11)\n\t" \
    "v_mfma_f32_16x16x32_bf16 %0, v[104:107], v[112:115], %0\n\t" \
    "ds_read_b128 v[100:103], %5 offset:1152\n\t" \
    "v_mfma_f32_16x16x32_bf16 %1, v[108:111], v[116:119], %1\n\t" \
    "ds_read_b128 v[104:107], %6 offset:1664\n\t" \
    "s_waitcnt lgkmcnt(11)\n\t" \
    "v_mfma_f32_16x16x32_bf16 %2, v[120:123], v[112:115], %2\n\t" \
    "ds_read_b128 v[108:111], %6 offset:1152\n\t" \
    "v_mfma_f32_16x16x32_bf16 %3, v[124:127], v[116:119], %3\n\t" \
    "ds_read_b128 v[112:115], %5 offset:640\n\t" \
    "s_waitcnt lgkmcnt(11)\n\t" \
    "v_mfma_f32_16x16x32_bf16 %0, a[80:83], v[64:67], %0\n\t" \
    "ds_read_b128 v[116:119], %5 offset:128\n\t" \
    "v_mfma_f32_16x16x32_bf16 %1, a[112:115], v[68:71], %1\n\t" \
    "ds_read_b128 v[120:123], %6 offset:640\n\t" \
    "s_waitcnt lgkmcnt(11)\n\t" \
    "v_mfma_f32_16x16x32_bf16 %2, v[72:75], v[64:67], %2\n\t" \
    "ds_read_b128 v[124:127], %6 offset:128\n\t" \
    "v_mfma_f32_16x16x32_bf16 %3, v[76:79], v[68:71], %3\n\t" \
    "ds_read_b128 v[64:67], %5 offset:1600\n\t" \
    "s_waitcnt lgkmcnt(11)\n\t" \
    "v_mfma_f32_16x16x32_bf16 %0, v[72:75], v[80:83], %0\n\t" \
    "ds_read_b128 v[68:71], %5 offset:1088\n\t" \
    "v_mfma_f32_16x16x32_bf16 %1, v[76:79], v[84:87], %1\n\t" \
    "ds_read_b128 v[72:75], %6 offset:1600\n\t" \
    "s_waitcnt lgkmcnt(11)\n\t" \
    "v_mfma_f32_16x16x32_bf16 %2, v[88:91], v[80:83], %2\n\t" \
    "ds_read_b128 v[76:79], %6 offset:1088\n\t" \
    "v_mfma_f32_16x16x32_bf16 %3, v[92:95], v[84:87], %3\n\t" \
    "ds_read_b128 v[80:83], %5 offset:576\n\t" \
    "s_waitcnt lgkmcnt(11)\n\t" \
    "v_mfma_f32_16x16x32_bf16 %0, a[84:87], v[96:99], %0\n\t" \
    "ds_read_b128 v[84:87], %5 offset:64\n\t" \
    "v_mfma_f32_16x16x32_bf16 %1, a[116:119], v[100:103], %1\n\t" \
    "ds_read_b128 v[88:91], %6 offset:576\n\t" \
    "s_waitcnt lgkmcnt(11)\n\t" \
    "v_mfma_f32_16x16x32_bf16 %2, v[104:107], v[96:99], %2\n\t" \
    "ds_read_b128 v[92:95], %6 offset:64\n\t" \
    "v_mfma_f32_16x16x32_bf16 %3, v[108:111], v[100:103], %3\n\t" \
    "ds_read_b128 v[96:99], %5 offset:1536\n\t" \
    "s_waitcnt lgkmcnt(11)\n\t" \
    "v_mfma_f32_16x16x32_bf16 %0, v[104:107], v[112:115], %0\n\t" \
    "ds_read_b128 v[100:103], %5 offset:1024\n\t" \
    "v_mfma_f32_16x16x32_bf16 %1, v[108:111], v[116:119], %1\n\t" \
    "ds_read_b128 v[104:107], %6 offset:1536\n\t" \
    "s_waitcnt lgkmcnt(11)\n\t" \
    "v_mfma_f32_16x16x32_bf16 %2, v[120:123], v[112:115], %2\n\t" \
    "ds_read_b128 v[108:111], %6 offset:1024\n\t" \
    "v_mfma_f32_16x16x32_bf16 %3, v[124:127], v[116:119], %3\n\t" \
    "ds_read_b128 v[112:115], %5 offset:512\n\t" \
    "s_waitcnt lgkmcnt(11)\n\t" \
    "v_mfma_f32_16x16x32_bf16 %0, a[88:91], v[64:67], %0\n\t" \
    "ds_read_b128 v[116:119], %5 offset:0\n\t" \
    "v_mfma_f32_16x16x32_bf16 %1, a[120:123], v[68:71], %1\n\t" \
    "ds_read_b128 v[120:123], %6 offset:512\n\t" \
    "s_waitcnt lgkmcnt(11)\n\t" \
    "v_mfma_f32_16x16x32_bf16 %2, v[72:75], v[64:67], %2\n\t" \
    "ds_read_b128 v[124:127], %6 offset:0\n\t" \
    "v_mfma_f32_16x16x32_bf16 %3, v[76:79], v[68:71], %3\n\t" \
    "s_waitcnt lgkmcnt(10)\n\t" \
    "v_mfma_f32_16x16x32_bf16 %0, v[72:75], v[80:83], %0\n\t" \
    "v_mfma_f32_16x16x32_bf16 %1, v[76:79], v[84:87], %1\n\t" \
    "s_waitcnt lgkmcnt(8)\n\t" \
    "v_mfma_f32_16x16x32_bf16 %2, v[88:91], v[80:83], %2\n\t" \
    "v_mfma_f32_16x16x32_bf16 %3, v[92:95], v[84:87], %3\n\t" \
    "s_waitcnt lgkmcnt(6)\n\t" \
    "v_mfma_f32_16x16x32_bf16 %0, a[92:95], v[96:99], %0\n\t" \
    "v_mfma_f32_16x16x32_bf16 %1, a[124:127], v[100:103], %1\n\t" \
    "s_waitcnt lgkmcnt(4)\n\t" \
    "v_mfma_f32_16x16x32_bf16 %2, v[104:107], v[96:99], %2\n\t" \
    "v_mfma_f32_16x16x32_bf16 %3, v[108:111], v[100:103], %3\n\t" \
    "s_waitcnt lgkmcnt(2)\n\t" \
    "v_mfma_f32_16x16x32_bf16 %0, v[104:107], v[112:115], %0\n\t" \
    "v_mfma_f32_16x16x32_bf16 %1, v[108:111], v[116:119], %1\n\t" \
    "s_waitcnt lgkmcnt(0)\n\t" \
    "v_mfma_f32_16x16x32_bf16 %2, v[120:123], v[112:115], %2\n\t" \
    "v_mfma_f32_16x16x32_bf16 %3, v[124:127], v[116:119], %3\n\t" \
    "s_nop 7\n\t" \
    "s_nop 1\n\t" \
    "s_waitcnt vmcnt(0)\n\t"

__device__ __forceinline__ float u2f(u32 x) { return __builtin_bit_cast(float, x); }
__device__ __forceinline__ u32 f2u(float x) { return __builtin_bit_cast(u32, x); }
__device__ __forceinline__ float blo(u32 x) { return u2f(x << 16); }
__device__ __forceinline__ float bhi(u32 x) { return u2f(x & 0xffff0000u); }
__device__ __forceinline__ u32 pkbf(float a, float b) {
    v2f f = {a, b};
    return __builtin_bit_cast(u32, __builtin_convertvector(f, v2bf));
}
__device__ __forceinline__ float rbf(float a) { return blo(pkbf(a, a)); }
__device__ __forceinline__ u32 pkh(float a, float b) {
    v2f f = {a, b};
    return __builtin_bit_cast(u32, __builtin_convertvector(f, v2h));
}
template <int CTRL> __device__ __forceinline__ float dppf(float x) {
    return __builtin_bit_cast(float, __builtin_amdgcn_mov_dpp(__builtin_bit_cast(int, x), CTRL, 0xf, 0xf, true));
}
template <int CTRL> __device__ __forceinline__ u32 dppu(u32 x) {
    return (u32)__builtin_amdgcn_mov_dpp((int)x, CTRL, 0xf, 0xf, true);
}
constexpr int XOR1 = 0xB1, XOR2 = 0x4E, HMIR = 0x141, MIR = 0x140;
__device__ __forceinline__ void bar() { asm volatile("s_waitcnt lgkmcnt(0)\n\ts_barrier" ::: "memory"); }
__device__ __forceinline__ void wait_vm0() { asm volatile("s_waitcnt vmcnt(0)" ::: "memory"); }
__device__ __forceinline__ u32 umin(u32 a, u32 b) { return a < b ? a : b; }
__device__ __forceinline__ u32 umax(u32 a, u32 b) { return a > b ? a : b; }
__device__ __forceinline__ u32 ns(u32 x) { return umin(x, SENT - 1u); }
__device__ __forceinline__ bool isvalid(v4u x) { return umax(umax(x.x, x.y), umax(x.z, x.w)) != SENT; }
template <class T> __device__ __forceinline__ void pin(T& x) { asm volatile("" : "+v"(x)); }

typedef __amdgpu_buffer_rsrc_t rsrc_t;
__device__ __forceinline__ rsrc_t mkr(const void* p, u32 bytes) { return __builtin_amdgcn_make_buffer_rsrc((void*)p, 0, bytes, 0x00020000); }
__device__ __forceinline__ void st16(rsrc_t r, u32 off, u32 so, v4u v) {
    asm volatile("" : "+v"(v));
    __builtin_amdgcn_raw_buffer_store_b128(v, r, off, so, 16);
    asm volatile("s_nop 1" :: "v"(v));
}
__device__ __forceinline__ void st8(rsrc_t r, u32 off, u32 so, v2u v) { __builtin_amdgcn_raw_buffer_store_b64(v, r, off, so, 16); }
__device__ __forceinline__ v4u ld16(rsrc_t r, u32 off, u32 so) { return __builtin_amdgcn_raw_buffer_load_b128(r, off, so, 16); }
__device__ __forceinline__ u32 ldw(rsrc_t r, u32 off, u32 so) { return __builtin_amdgcn_raw_buffer_load_b32(r, off, so, 0); }
__device__ __forceinline__ u32 ldh(rsrc_t r, u32 off, u32 so) { return (u32)__builtin_amdgcn_raw_buffer_load_b16(r, off, so, 0); }

__device__ __forceinline__ void wait_flag(rsrc_t r, u32 off, v2u tag, bool& dead) {
    u32 spins = 0;
    while (!dead) {
        asm volatile("" ::: "memory");
        const v2u x = __builtin_amdgcn_raw_buffer_load_b64(r, off, 0, 16);
        if (x.x == tag.x && x.y == tag.y) break;
        if (++spins > SPIN_CAP) dead = true;
    }
}
__device__ __forceinline__ void repoll16(rsrc_t r, u32 off, u32 so, v4u& x, bool& dead) {
    u32 spins = 0;
    while (!isvalid(x) && !dead) {
        asm volatile("" ::: "memory");
        x = ld16(r, off, so);
        if (++spins > SPIN_CAP) dead = true;
    }
}

__device__ __forceinline__ void build_T(char* base, v4u lo, v4u hi) {
    const u32 W[8] = {lo.x, lo.y, lo.z, lo.w, hi.x, hi.y, hi.z, hi.w};
    u32 R[8], B[8];
#pragma unroll
    for (int m = 1; m < 8; ++m) {
        R[m] = __builtin_amdgcn_alignbit(W[m], W[m], 16);
        B[m] = (W[m] & 0xffffu) | (W[m - 1] & 0xffff0000u);
    }
#pragma unroll
    for (int rho = 0; rho < 8; ++rho) {
        const int idx = rho < 4 ? 2 * rho : 2 * (rho - 4) + 1;
        v4u o;
        if (rho & 1) { const int m = (7 + rho) / 2; o = (v4u){R[m], R[m - 1], R[m - 2], R[m - 3]}; }
        else { const int m = 4 + rho / 2; o = (v4u){B[m], B[m - 1], B[m - 2], B[m - 3]}; }
        *(v4u*)(base + idx * 258 * 16) = o;
    }
}

__device__ __forceinline__ void to_rowlay(v4u a, u32 (&o)[4]) {
    auto s0 = __builtin_amdgcn_permlane32_swap(a.x, a.z, false, false);
    auto s1 = __builtin_amdgcn_permlane32_swap(a.y, a.w, false, false);
    auto s2 = __builtin_amdgcn_permlane16_swap(s0[0], s1[0], false, false);
    auto s3 = __builtin_amdgcn_permlane16_swap(s0[1], s1[1], false, false);
    o[0] = s2[0]; o[1] = s2[1]; o[2] = s3[0]; o[3] = s3[1];
}
__device__ __forceinline__ v4u from_rowlay(const u32 (&o)[4]) {
    auto s2 = __builtin_amdgcn_permlane16_swap(o[0], o[1], false, false);
    auto s3 = __builtin_amdgcn_permlane16_swap(o[2], o[3], false, false);
    auto s0 = __builtin_amdgcn_permlane32_swap(s2[0], s3[0], false, false);
    auto s1 = __builtin_amdgcn_permlane32_swap(s2[1], s3[1], false, false);
    return (v4u){s0[0], s1[0], s0[1], s1[1]};
}

__device__ __forceinline__ void lsig(u32 addr) {
    asm volatile("s_mov_b64 exec, 1\n\tds_add_u32 %0, %1\n\ts_mov_b64 exec, -1" :: "v"(addr), "v"(LINC) : "memory");
}
#define SETPRIO(x) __builtin_amdgcn_s_setprio(x)
__device__ __forceinline__ void lwait(const char* p, u32 target) {
    u32 tg = target * LINC + 1u;
    do {
        asm volatile("" ::: "memory");
        --tg;
    } while ((u32)__builtin_amdgcn_readfirstlane(*(const u32*)p) < tg);
    asm volatile("" ::: "memory");
}
__device__ __forceinline__ bool lpeek(const char* p, u32 target) {
    asm volatile("" ::: "memory");
    return (u32)__builtin_amdgcn_readfirstlane(*(const u32*)p) >= target * LINC;
}
#define POLL_HEAD "s_mov_b64 vcc, -1\n\t1:\n\ts_mov_b64 exec, vcc\n\t"
#define POLL_MID "s_mov_b64 exec, -1\n\ts_sub_i32 %0, %0, 1\n\ts_cmp_gt_i32 %0, 0\n\ts_cselect_b64 %1, -1, 0\n\t"
#define POLL_TAIL "s_and_b64 vcc, vcc, %1\n\ts_cbranch_scc1 1b\n\t"
__device__ __forceinline__ void poll2(rsrc_t ws, u32 off, u32 so0, u32 so1, v4u& a, v4u& b, int& bud) {
    u64 m; u32 t;
    asm volatile(POLL_HEAD
        "buffer_load_dwordx4 v[56:59], %5, %6, %7 offen sc1\n\t"
        "buffer_load_dwordx4 v[60:63], %5, %6, %8 offen sc1\n\t"
        POLL_MID
        "s_waitcnt vmcnt(1)\n\t"
        "v_max3_u32 %2, v56, v57, v58\n\t"
        "s_waitcnt vmcnt(0)\n\t"
        "v_max3_u32 %2, v59, v60, %2\n\t"
        "v_max3_u32 %2, v61, v62, %2\n\t"
        "v_max_u32 %2, v63, %2\n\t"
        "v_cmp_eq_u32 vcc, -1, %2\n\t"
        POLL_TAIL
        : "+s"(bud), "=&s"(m), "=&v"(t), "=&{v[56:59]}"(a), "=&{v[60:63]}"(b) : "v"(off), "s"(ws), "s"(so0), "s"(so1) : "memory", "vcc", "scc");
}
__device__ __forceinline__ void poll1(rsrc_t ws, u32 off, u32 so, v4u& a, int& bud) {
    u64 m; u32 t;
    asm volatile(POLL_HEAD
        "buffer_load_dwordx4 v[56:59], %4, %5, %6 offen sc1\n\t"
        POLL_MID
        "s_waitcnt vmcnt(0)\n\t"
        "v_max3_u32 %2, v56, v57, v58\n\t"
        "v_max_u32 %2, v59, %2\n\t"
        "v_cmp_eq_u32 vcc, -1, %2\n\t"
        POLL_TAIL
        : "+s"(bud), "=&s"(m), "=&v"(t), "=&{v[56:59]}"(a) : "v"(off), "s"(ws), "s"(so) : "memory", "vcc", "scc");
}
__device__ __forceinline__ void pollf(rsrc_t ws, u32 off, u32 so, u64 f, u64 tag, int& bud) {
    if (__builtin_amdgcn_ballot_w64(f != tag) != 0) {
        u64 m;
        asm volatile(POLL_HEAD
            "buffer_load_dwordx2 %2, %3, %4, %5 offen sc1\n\t"
            POLL_MID
            "s_waitcnt vmcnt(0)\n\t"
            "v_cmp_ne_u64 vcc, %6, %2\n\t"
            POLL_TAIL
            : "+s"(bud), "=&s"(m), "=&v"(f) : "v"(off), "s"(ws), "s"(so), "s"(tag) : "memory", "vcc", "scc");
    }
}
__device__ __forceinline__ u64 ldf(rsrc_t ws, u32 off, u32 so) { return __builtin_bit_cast(u64, __builtin_amdgcn_raw_buffer_load_b64(ws, off, so, 16)); }

struct Stat { float ma, mb, ra, rb; };
__device__ __forceinline__ void row_stats(char* rr, int r, int lane, const float (&da)[8], const float (&db)[8], Stat& s, float& na, float& nb) {
    float v0 = ((da[0] + da[1]) + (da[2] + da[3])) + ((da[4] + da[5]) + (da[6] + da[7]));
    float v2 = ((db[0] + db[1]) + (db[2] + db[3])) + ((db[4] + db[5]) + (db[6] + db[7]));
    float v1 = da[0] * da[0], v1b = da[1] * da[1], v3 = db[0] * db[0], v3b = db[1] * db[1];
#pragma unroll
    for (int k = 2; k < 8; k += 2) {
        v1 = __builtin_fmaf(da[k], da[k], v1); v1b = __builtin_fmaf(da[k + 1], da[k + 1], v1b);
        v3 = __builtin_fmaf(db[k], db[k], v3); v3b = __builtin_fmaf(db[k + 1], db[k + 1], v3b);
    }
    v1 += v1b; v3 += v3b;
    asm volatile(
        "s_nop 1\n\t"
        "v_add_f32_dpp %0, %0, %0 row_ror:8 row_mask:0xf bank_mask:0x3\n\t"
        "v_add_f32_dpp %1, %1, %1 row_ror:8 row_mask:0xf bank_mask:0x3\n\t"
        "v_add_f32_dpp %0, %2, %2 row_ror:8 row_mask:0xf bank_mask:0xc\n\t"
        "v_add_f32_dpp %1, %3, %3 row_ror:8 row_mask:0xf bank_mask:0xc\n\t"
        "s_nop 0\n\t"
        "v_add_f32_dpp %0, %0, %0 row_half_mirror row_mask:0xf bank_mask:0x5\n\t"
        "v_add_f32_dpp %0, %1, %1 row_half_mirror row_mask:0xf bank_mask:0xa\n\t"
        "s_nop 1\n\t"
        "v_add_f32_dpp %0, %0, %0 quad_perm:[1,0,3,2] row_mask:0xf bank_mask:0xf\n\t"
        "s_nop 1\n\t"
        "v_add_f32_dpp %0, %0, %0 quad_perm:[2,3,0,1] row_mask:0xf bank_mask:0xf\n\t"
        : "+v"(v0), "+v"(v1) : "v"(v2), "v"(v3));
    *(u32*)(rr + (lane >> 4) * 64 + r * 16 + (lane & 12)) = ns(f2u(v0));
    v4u zi;
    u32 spins = 0;
    do {
        asm volatile("" ::: "memory");
        zi = *(const v4u*)(rr + (lane >> 4) * 64 + (lane & 3) * 16);
    } while (__builtin_amdgcn_ballot_w64(!isvalid(zi)) != 0 && ++spins < LSPIN_CAP);
    v4f z = __builtin_bit_cast(v4f, zi);
#pragma unroll
    for (int k = 0; k < 4; ++k) z[k] += dppf<XOR1>(z[k]);
#pragma unroll
    for (int k = 0; k < 4; ++k) z[k] += dppf<XOR2>(z[k]);
    const float inv = 1.f / DM;
    const float mua = z[0] * inv, mub = z[2] * inv;
    const float va = __builtin_fmaxf(__builtin_fmaf(-mua, mua, z[1] * inv), 0.f), vb = __builtin_fmaxf(__builtin_fmaf(-mub, mub, z[3] * inv), 0.f);
    const u32 pv = pkbf(va, vb);
    const u32 pe = pkbf(blo(pv) + 1e-5f, bhi(pv) + 1e-5f);
    const u32 pd = pkbf(__builtin_amdgcn_sqrtf(blo(pe)), __builtin_amdgcn_sqrtf(bhi(pe)));
    const u32 pm = pkbf(s.ma + mua, s.mb + mub);
    s.ra = __builtin_amdgcn_rcpf(blo(pd));
    s.rb = __builtin_amdgcn_rcpf(bhi(pd));
    na = (s.ma - blo(pm)) * s.ra;
    nb = (s.mb - bhi(pm)) * s.rb;
    s.ma = blo(pm);
    s.mb = bhi(pm);
}
#define MFMA(a, b, c) __builtin_amdgcn_mfma_f32_16x16x32_bf16(a, b, c, 0, 0, 0)
#define SGB(m, n) __builtin_amdgcn_sched_group_barrier(m, n, 0)
__device__ __forceinline__ u32 ln2(u32 x, const Stat& s) {
    const u32 pd = pkbf(blo(x) - s.ma, bhi(x) - s.mb);
    return pkbf(blo(pd) * s.ra, bhi(pd) * s.rb);
}
__device__ __forceinline__ u32 add2(u32 a, u32 b) { return pkbf(blo(a) + blo(b), bhi(a) + bhi(b)); }
__device__ __forceinline__ u32 mul2(u32 a, u32 b) { return pkbf(blo(a) * blo(b), bhi(a) * bhi(b)); }
__device__ __forceinline__ v4u pair_xpose(bool odd, u32 c0a, u32 c0b, u32 c1a, u32 c1b) {
    const u32 sa = odd ? c0a : c1a, sb = odd ? c0b : c1b;
    const u32 ra = dppu<XOR1>(sa), rb = dppu<XOR1>(sb);
    return (v4u){odd ? ra : c0a, odd ? rb : c0b, odd ? c1a : ra, odd ? c1b : rb};
}

struct cf { float r, i; };
__device__ __forceinline__ cf cmul(cf a, cf b) { return {__builtin_fmaf(a.r, b.r, -(a.i * b.i)), __builtin_fmaf(a.r, b.i, a.i * b.r)}; }
template <int K> __device__ __forceinline__ cf cexpk(float la, float th, float tl) {
    const float sc = (float)(1 << K);
    const float mag = __builtin_amdgcn_exp2f(la * sc);
    const float ph = __builtin_fmaf(tl, sc, __builtin_amdgcn_fractf(th * sc));
    return {mag * __builtin_amdgcn_cosf(ph), mag * __builtin_amdgcn_sinf(ph)};
}
__device__ __forceinline__ float exp_acc(float x) {
    const float y = x * 1.44269504088896340736f;
    const float n = __builtin_rintf(y);
    float f = __builtin_fmaf(x, 1.44269504088896340736f, -n);
    f = __builtin_fmaf(x, 1.92596299e-8f, f);
    const float nc = __builtin_fminf(__builtin_fmaxf(n, -300.f), 300.f);
    const float e = __builtin_ldexpf(__builtin_amdgcn_exp2f(f), (int)nc);
    return __builtin_fabsf(y) < 400.f ? e : __builtin_amdgcn_exp2f(y);
}

#define DLY_U0 210
#define DLY_U1 300
#define DLY_Y 20
#define DLY_K 6
#define DLY_S 4
#define SSMK_POS 0
#define Q_POS 0
#define YC_POS 1
#define CPRIO 1
#define RPRIO 0
#define XPRIO 0
#define ORDER 1
#define PRE_ADV 95
#define NOROW 0
#define YL 0
struct KG { float la, th, tl, dt; };
struct KP { u32 a, im, dt; };
__device__ __forceinline__ KP kg_load(const Params& p, int chan, int kn) {
    KP k;
    const u32 so = (u32)chan * (NST * 2);
    k.a = ldh(mkr(p.logA, DM * NST * 2), kn * 2, so);
    k.dt = ldh(mkr(p.logdt, DM * 2), 0, chan * 2);
    k.im = ldh(mkr(p.Aim, DM * NST * 2), kn * 2, so);
    return k;
}
__device__ __forceinline__ KG kg_prep(const KP& k) {
    KG g;
    g.dt = rbf(exp_acc(blo(k.dt)));
    const float zr = -rbf(exp_acc(blo(k.a))) * g.dt;
    const float zi = blo(k.im) * g.dt;
    g.la = zr * 1.44269504088896340736f;
    g.th = zi * 0.15915494309189533577f;
    g.tl = __builtin_fmaf(zi, 6.4206383e-9f, __builtin_fmaf(zi, 0.15915494309189533577f, -g.th));
    return g;
}
#define FENCE() asm volatile("" ::: "memory")
#define PUTH(row, h) do { const cf h_ = (h); *(u32*)(scr + (row) * SCR_ROW) = pkh(h_.r, -h_.i); } while (0)
#define PUTL(row, l) do { const cf l_ = (l); *(u32*)(scr + (41 + (row)) * SCR_ROW) = pkh(l_.r, l_.i); } while (0)
#define CEXP(k) cexpk<k>(g.la, g.th, g.tl)
#define PUTP(row, v) (*(u32*)(scr + (row) * SCR_ROW) = (v))
__device__ __forceinline__ u32 pkc(cf a) { return pkh(a.r, -a.i); }
__device__ __forceinline__ u32 pkp(cf a) { return pkh(a.r, a.i); }
__device__ __forceinline__ u32 hmul(u32 x, u32 y) {
    u32 t;
    asm("v_pk_mul_f16 %0, %1, %2 op_sel:[0,0] op_sel_hi:[0,1]\n\t"
        "v_pk_fma_f16 %0, %1, %2, %0 op_sel:[1,1,0] op_sel_hi:[1,0,1] neg_lo:[0,1,0]" : "=&v"(t) : "v"(x), "v"(y));
    return t;
}
#define DSK(k) (((k) & 2) ? bhi(Dsk[((k) >> 2) * 2 + ((k) & 1)]) : blo(Dsk[((k) >> 2) * 2 + ((k) & 1)]))
__device__ __forceinline__ v4u half_xpose(u32 c0a, u32 c0b, u32 c1a, u32 c1b) {
    auto sa = __builtin_amdgcn_permlane32_swap(c0a, c1a, false, false);
    auto sb = __builtin_amdgcn_permlane32_swap(c0b, c1b, false, false);
    return (v4u){sa[0], sb[0], sa[1], sb[1]};
}

__device__ __forceinline__ void get_y(rsrc_t ws, u32 off, u32 so0, u32 so1, u32 (&y)[8], int& dead) {
    v4u y0, y1;
    poll2(ws, off, so0, so1, y0, y1, dead);
    u32 o[4];
    to_rowlay(y0, o);
#pragma unroll
    for (int k = 0; k < 4; ++k) y[k] = o[k];
    to_rowlay(y1, o);
#pragma unroll
    for (int k = 0; k < 4; ++k) y[4 + k] = o[k];
}
__device__ __forceinline__ void put_u(rsrc_t ws, u32 off, u32 so, const float (&da)[8], const float (&db)[8], const Stat& s, float na, float nb) {
#pragma unroll
    for (int e = 0; e < 2; ++e) {
        u32 uh[4];
#pragma unroll
        for (int k = 0; k < 4; ++k) uh[k] = ns(pkbf(__builtin_fmaf(da[e * 4 + k], s.ra, na), __builtin_fmaf(db[e * 4 + k], s.rb, nb)));
        st16(ws, off, so + e * 262144u, from_rowlay(uh));
    }
}

__global__ void __launch_bounds__(512, 2) mega(Params p) {
    __shared__ __attribute__((aligned(16))) char smem[L_TOTAL];
    const int t = __builtin_amdgcn_workitem_id_x(), lane = t & 63;
    const int w = __builtin_amdgcn_readfirstlane(t >> 6);
    const int b = __builtin_amdgcn_workgroup_id_x();
    const int cid = (b & 7) * 32 + (b >> 3);
    const int half = b >> 7;
    const int rid = half * 128 + (b & 7) * 16 + ((b >> 3) & 15);
    const rsrc_t ws = mkr(p.ws, WS_BYTES);
    int dead = SPIN_CAP;
    const v4u sent4 = {SENT, SENT, SENT, SENT};
#define CLR(o, so, lvl) st16(ws, o, so, sent4)
    const int li = lane & 15, lg = lane >> 4;
    const u32 sbase = (u32)(size_t)(LAS char*)smem;
    STAMP(0);
    asm volatile("" :: "s"(p.q), "s"(p.k), "s"(p.v), "s"(p.sk), "s"(p.ssmk), "s"(p.ssmD), "s"(p.logA), "s"(p.Aim), "s"(p.Cr), "s"(p.Ci), "s"(p.logdt), "s"(p.Dskip), "s"(p.out));
    u64 ep = *(const __attribute__((address_space(4))) u64*)(p.ws + WS_EPOCH);
#define BAR_EP() asm volatile("s_waitcnt lgkmcnt(0)\n\ts_barrier" : "+s"(ep) :: "memory")
    const int kch = (w >> 1) & 1, kn = (w & 1) * 64 + lane;
    char* scr = smem + L_SCR + kch * SCR_CH + kn * 4;
    const int slot = cid * 2 + kch;
    const int chan = (slot & ~63) + 2 * (slot & 31) + ((slot >> 5) & 1);
    const u32 o_w = (u32)lg * 65536u + (u32)li * 16u;
    const u32 s_w = WS_U + (u32)rid * 256u + (u32)(w & 3) * 524288u;
    const u32 o_yc = (u32)((lane >> 3) & 3) * 65536u + (u32)(lane >> 5) * 128u + (u32)(lane & 7) * 16u;
    const u32 s_yc = WS_Y + (u32)w * 262144u + (u32)cid * 256u;
    const u32 s_yw = WS_Y + (u32)(w & 1) * 131072u + (u32)slot * 128u;
#define TAG() u64 e1_ = ep; asm volatile("" : "+s"(e1_)); e1_ += 1ull; const v2u tag = {(u32)e1_, (u32)(e1_ >> 32)}
    if (w < 4) {
        const int ch = w >> 1, par = w & 1;
        CLR(o_w, s_w, 3);
        CLR(o_w, s_w + 262144u, 3);
        const KP kp = kg_load(p, chan, kn);
        u32 pcr, pci, pcr2, pci2;
        {
            const rsrc_t rcr = mkr(p.Cr, DM * NST * 2), rci = mkr(p.Ci, DM * NST * 2);
            pcr = ldh(rcr, kn * 2, chan * (NST * 2)); pci = ldh(rci, kn * 2, chan * (NST * 2));
            pcr2 = ldh(rcr, (kn ^ 64) * 2, chan * (NST * 2)); pci2 = ldh(rci, (kn ^ 64) * 2, chan * (NST * 2));
        }
        FENCE();
        v4u sa0[2], sa1[2];
        auto late_loads = [&]() {
            FENCE();
        {
            const rsrc_t rs = mkr(p.ssmk + (size_t)chan * SL, SL * 2);
            const u32 o = (u32)(255 - (t & 127)) * 16u;
            sa1[0] = __builtin_amdgcn_raw_buffer_load_b128(rs, o, 0, 0);
            sa0[0] = __builtin_amdgcn_raw_buffer_load_b128(rs, o - 16u, 0, 0);
            sa1[1] = __builtin_amdgcn_raw_buffer_load_b128(rs, o - 2048u, 0, 0);
            sa0[1] = __builtin_amdgcn_raw_buffer_load_b128(rs, o - 2064u, 0, 0);
        }
        FENCE();
        };
        if (ORDER == 4) late_loads();
        *(v4u*)(smem + L_U + w * U_CH + lane * 16) = (v4u){0u, 0u, 0u, 0u};
        {
            const u32 ia = w == 0 ? L_RR + lane * 16 : w == 1 ? L_CNT + li * 16 : w == 2 ? L_T + (li >> 3) * T_CH + ((li & 7) * 258 + 257) * 16 : L_KL + (lane & 1) * KL_CH;
            const u32 iv = w == 0 ? SENT : 0u;
            *(v4u*)(smem + ia) = (v4u){iv, iv, iv, iv};
        }
        BAR_EP();
        TAG();
        const u32 o_fs = lane == 0 ? WS_FLAG : 0x80000000u;
#define STFLAG(so) st8(ws, o_fs, so, tag)
        asm volatile("s_waitcnt vmcnt(7)" ::: "memory");
        lsig(sbase + C_K1);
        STFLAG((w * 256 + cid) * 8);
        STAMP(7);
        const u32 pf1 = ldw(ws, (u32)lane * MB2 + (WS_U - 128u), 0);
        const u32 pf2 = ldw(mkr(p.out, SL * DM * 2), 0, rid * 8192);
        auto y0_clear = [&]() {
            FENCE();
            CLR(o_yc, s_yc, 2);
            CLR(o_yc, s_yc + MB1, 2);
            FENCE();
        };
        if (ORDER != 3) y0_clear();
        if (ORDER != 2 && ORDER != 4) late_loads();
        SETPRIO(CPRIO);
        {
            const KG g = kg_prep(kp);
            u32 cm = umax(umax(pcr & 0x7fffu, pci & 0x7fffu), umax(pcr2 & 0x7fffu, pci2 & 0x7fffu));
            cm = umax(cm, dppu<XOR1>(cm));
            cm = umax(cm, dppu<XOR2>(cm));
            cm = umax(cm, dppu<HMIR>(cm));
            cm = umax(cm, dppu<MIR>(cm));
            { auto sw = __builtin_amdgcn_permlane16_swap(cm, cm, false, false); cm = umax(sw[0], sw[1]); }
            { auto sw = __builtin_amdgcn_permlane32_swap(cm, cm, false, false); cm = umax(sw[0], sw[1]); }
            const int e = (int)(cm >> 7);
            const int sf = 267 - e < 253 ? 267 - e : 253;
            const float scale = u2f((u32)sf << 23);
            *(float*)(smem + L_YSC + kch * 4) = 2.f * g.dt * u2f((u32)(254 - sf) << 23);
            cf G[4], F[8];
            G[0] = {blo(pcr) * scale, blo(pci) * scale};
            const cf E7 = CEXP(7), E8 = CEXP(8), E9 = CEXP(9);
            G[1] = cmul(G[0], E7); G[2] = cmul(G[0], E8); G[3] = cmul(G[1], E8);
            F[1] = CEXP(0); F[2] = CEXP(1); F[4] = CEXP(2);
            F[3] = cmul(F[1], F[2]); F[5] = cmul(F[1], F[4]); F[6] = cmul(F[2], F[4]); F[7] = cmul(F[3], F[4]);
            u32 gp[4], fp[8];
#pragma unroll
            for (int a = 0; a < 4; ++a) gp[a] = pkc(G[a]);
#pragma unroll
            for (int a = 1; a < 8; ++a) fp[a] = pkc(F[a]);
            PUTP(0, gp[0]); PUTP(8, gp[1]); PUTP(16, gp[2]); PUTP(20, gp[3]);
            if (ORDER == 3) y0_clear();
            if (ORDER == 2) late_loads();
#pragma unroll
            for (int e2 = 1; e2 < 8; ++e2) { PUTP(e2, hmul(gp[0], fp[e2])); PUTP(8 + e2, hmul(gp[1], fp[e2])); }
#pragma unroll
            for (int e2 = 1; e2 < 4; ++e2) { PUTP(16 + e2, hmul(gp[2], fp[2 * e2])); PUTP(20 + e2, hmul(gp[3], fp[2 * e2])); }
            const u32 e9 = pkc(E9), e10 = pkc(CEXP(10));
#pragma unroll
            for (int a = 0; a < 4; ++a) {
                const u32 h = pkc(cmul(G[a], E9));
                PUTP(24 + 2 * a, h); PUTP(25 + 2 * a, hmul(h, fp[4])); PUTP(32 + a, hmul(h, e9)); PUTP(36 + a, hmul(h, e10));
            }
            PUTP(40, hmul(gp[0], pkc(CEXP(11))));
        }
        lsig(sbase + C_K);
        STAMP(1);
        const u32 o_fl1 = WS_FLAG + (u32)((cid >> 6) * 256 + (t >> 5) * 32 + (t & 1) * 16 + ((t >> 1) & 15)) * 8u;
        asm volatile("s_waitcnt vmcnt(4)" ::: "memory");
        if (ORDER != 3) STFLAG((w * 256 + cid) * 8 + 8192);
        asm volatile("" :: "v"(pf1), "v"(pf2));
        char* tslot = smem + L_T + ch * T_CH + ((t & 127) + 1) * 16;
        build_T(tslot, sa0[0], sa1[0]);
        FENCE();
        u64 fl = ldf(ws, o_fl1, 0);
        FENCE();
        build_T(tslot + 2048, sa0[1], sa1[1]);
        lsig(sbase + C_CONV);
        const int rho = li & 7;
        const u32 tb = sbase + L_T + ch * T_CH + ((rho < 4 ? 2 * rho : 2 * (rho - 4) + 1) * 258 + 2 - (li >> 3) + lg) * 16;
        const u32 ub = sbase + L_U + ch * U_CH + (4 * (li & 8) + 16 * par + 2 * (li & 7) + lg + 2) * 16;
        const u32 o_yw = (u32)(lg & 1) * 524288u + (u32)(li & 8) * 32768u + (u32)(li & 4) * 16384u + (u32)(li & 3) * 32u + (u32)(lg & 2) * 8u;
        const u32 o_ur = (u32)(t >> 1) * 256u + (u32)(t & 1) * 16u;
        const u32 s_ur = WS_U + (u32)(cid >> 3) * 65536u + (u32)(cid & 7) * 32u;
        char* uslot = smem + L_U + (t & 1) * U_CH + (64 + (t >> 1)) * 16;
        u32 tq = (u32)__builtin_amdgcn_s_memrealtime();
        STAMP(26);
        lwait(smem + C_CONV, 4);
        STAMP(2);
        asm volatile(TLOAD_ASM :: "v"(tb) : "memory", TLOAD_CLOB);
        pollf(ws, o_fl1, 0, fl, e1_, dead);
        if (NOROW) __builtin_amdgcn_s_sleep(NOROW);
        v4u uin;
        u32 tg = 8 * LINC + 1u;
#define PUT_Y(so) do { \
            a0 += a1; a2 += a3; \
            auto s0 = __builtin_amdgcn_permlane16_swap(ns(pkbf(a0[0], a0[1])), ns(pkbf(a2[0], a2[1])), false, false); \
            auto s1 = __builtin_amdgcn_permlane16_swap(ns(pkbf(a0[2], a0[3])), ns(pkbf(a2[2], a2[3])), false, false); \
            st16(ws, o_yw, so, (v4u){s0[0], s1[0], s0[1], s1[1]}); \
            lsig(sbase + C_Y); } while (0)
#define TRI(uu) do { u32 t1_ = tg, t2_; \
            asm volatile(TRI_ASM : "+v"(a0), "+v"(a1), "+v"(a2), "+v"(a3), "+s"(t1_), "=&s"(t2_) : "v"(sbase + C_CONV), "v"(uu) : "memory", "scc", CONV_CLOB); \
            tg += 4 * LINC; } while (0)
#pragma unroll 1
        for (int L = 0;; ++L) {
            const int uo = (L & 1) * 2 * U_CH;
            const u32 lo = (u32)L * MB2;
            const u32 u = ub + uo;
            if (L) STAMP(3 + L * 8);
            poll1(ws, o_ur, s_ur + lo, uin, dead);
            *(v4u*)(uslot + uo) = uin;
            lsig(sbase + C_CONV);
            u64 tn = __builtin_amdgcn_s_memrealtime();
            const u32 oob = L == 2 ? 0x80000000u : 0u;
            CLR(o_w | oob, s_w + lo + MB2, 1);
            CLR(o_w | oob, s_w + lo + MB2 + 262144u, 1);
            v4f a0 = {0.f, 0.f, 0.f, 0.f}, a1 = a0, a2 = a0, a3 = a0;
            STAMP(4 + L * 8);
            TRI(u);
            PUT_Y(s_yw + lo);
            STAMP(5 + L * 8);
            CLR(o_yc | oob, s_yc + lo + MB2, 1);
            CLR(o_yc | oob, s_yc + lo + MB2 + MB1, 1);
            a0 = (v4f){0.f, 0.f, 0.f, 0.f}; a1 = a0; a2 = a0; a3 = a0;
            asm volatile("" : "+s"(tn));
            const bool pre = (int)((u32)tn + PRE_ADV - tq) >= 0;
            asm volatile(CROSS_ASM : "+v"(a0), "+v"(a1), "+v"(a2), "+v"(a3), "=&v"(uin)
                         : "v"(u), "v"(tb), "v"(pre ? o_ur : 0x80000000u), "s"(ws), "s"(s_ur + lo + 32768u) : "memory", CONV_CLOB);
            lsig(sbase + C_CL);
            STAMP(6 + L * 8);
            if (!pre || __builtin_amdgcn_ballot_w64(!isvalid(uin)) != 0) {
                u32 n = 0;
#pragma nounroll
                while ((int)((u32)__builtin_amdgcn_s_memrealtime() - tq) < 0 && ++n < 1000) __builtin_amdgcn_s_sleep(1);
                poll1(ws, o_ur, s_ur + lo + 32768u, uin, dead);
            }
            *(v4u*)(uslot + uo + 2048) = uin;
            lsig(sbase + C_CONV);
            STFLAG((w * 256 + cid) * 8 + 16384 + L * 8192);
            STAMP(8 + L * 8);
            TRI(u + 2048);
            PUT_Y(s_yw + lo + MB1);
            STAMP(9 + L * 8);
            if (L == 2) break;
            tq = (u32)__builtin_amdgcn_s_memrealtime() + (L == 0 ? DLY_U0 : DLY_U1);
            if (L == 0) {
                lwait(smem + C_ROW, 12);
                STAMP(10);
                const char* kl = smem + L_KL + ch * KL_CH + (127 - (t & 127)) * 16;
                build_T(tslot, *(const v4u*)(kl + 2048), *(const v4u*)(kl + 2064));
                build_T(tslot + 2048, *(const v4u*)kl, *(const v4u*)(kl + 16));
                lsig(sbase + C_CONV);
                tg += 4 * LINC;
                lwait(smem + C_CONV, 16);
                asm volatile(TLOAD_ASM :: "v"(tb) : "memory", TLOAD_CLOB);
                STAMP(27);
            }
            fl = ldf(ws, o_fl1, 16384 + L * 8192);
            pollf(ws, o_fl1, 16384 + L * 8192, fl, e1_, dead);
            lwait(smem + C_RY, 4 * L + 4);
        }
        STAMP_OUT();
        return;
    }
    const int r = w - 4;
    SETPRIO(3);
    const u32 o_rw = (u32)(lane >> 5) * 4096u + (u32)(lane & 31) * 4u;
    const u32 s_rw = (u32)rid * 8192u + (u32)r * 256u;
    u32 kk[2][7], vv[2][4], Dch[2];
    v4u sk4[2];
    const KP kp = kg_load(p, chan, kn);
    FENCE();
    {
        const rsrc_t rk = mkr(p.k, SL * DM * 2), rv = mkr(p.v, SL * DM * 2);
        const u32 ok = o_rw + s_rw - 3072u;
#pragma unroll
        for (int e = 0; e < 2; ++e) {
#pragma unroll
            for (int j = 0; j < 3; ++j) kk[e][j] = ldw(rk, ok + j * 1024u + e * 128u, 0);
#pragma unroll
            for (int j = 0; j < 4; ++j) kk[e][3 + j] = ldw(rk, o_rw + j * 1024u + e * 128u, s_rw);
#pragma unroll
            for (int j = 0; j < 4; ++j) vv[e][j] = ldw(rv, o_rw + j * 1024u + e * 128u, s_rw);
            sk4[e] = __builtin_amdgcn_raw_buffer_load_b128(mkr(p.sk, DM * 8), (lane & 31) * 16 + e * 512, r * 1024, 0);
            Dch[e] = ldh(mkr(p.ssmD, DM * 2), (lane & 31) * 4 + (lane >> 5) * 2 + e * 128, r * 256);
        }
    }
    STAMP(6);
    BAR_EP();
    SETPRIO(RPRIO);
    {
        const KG g = kg_prep(kp);
        cf L[9];
        L[1] = CEXP(3); L[2] = CEXP(4); L[4] = CEXP(5); L[8] = CEXP(6);
        L[3] = cmul(L[1], L[2]); L[5] = cmul(L[1], L[4]); L[6] = cmul(L[2], L[4]); L[7] = cmul(L[3], L[4]);
        PUTP(41, 0x3c00u);
        const u32 l8 = pkp(L[8]);
#pragma unroll
        for (int i = 1; i < 8; ++i) { const u32 l = pkp(L[i]); PUTP(41 + i, l); PUTP(49 + i, hmul(l, l8)); }
        PUTP(49, l8);
    }
    lsig(sbase + C_K);
    asm volatile("" : "+v"(kk[0][0]), "+v"(kk[0][1]), "+v"(kk[0][2]), "+v"(kk[0][3]), "+v"(kk[0][4]), "+v"(kk[0][5]), "+v"(kk[0][6]), "+v"(sk4[0]) :: "memory");
    SETPRIO(3);
    STAMP(20);
    u32 qq[2][4], pil, Dsk[4];
    auto q_loads = [&]() {
    {
        const rsrc_t rq = mkr(p.q, SL * DM * 2);
#pragma unroll
        for (int e = 0; e < 2; ++e) {
#pragma unroll
            for (int j = 0; j < 4; ++j) qq[e][j] = ldw(rq, o_rw + j * 1024u + e * 128u, s_rw);
        }
        pil = ldh(rq, lg * 2048, rid * 8192) | (ldh(rq, lg * 2048 + 1024, rid * 8192) << 16);
    }
    {
        const rsrc_t rd2 = mkr(p.Dskip, DM * 2);
#pragma unroll
        for (int k = 0; k < 4; ++k) Dsk[k] = ldw(rd2, li * 4 + k * 64, r * 256);
    }
        FENCE();
    };
    if (Q_POS == 0) q_loads();
    FENCE();
    v4u kvp[2];
    {
#pragma unroll
        for (int e = 0; e < 2; ++e) {
            const float w00 = blo(sk4[e].x), w01 = bhi(sk4[e].x), w02 = blo(sk4[e].y), w03 = bhi(sk4[e].y);
            const float w10 = blo(sk4[e].z), w11 = bhi(sk4[e].z), w12 = blo(sk4[e].w), w13 = bhi(sk4[e].w);
            float a0[4], a1[4];
#pragma unroll
            for (int j = 0; j < 4; ++j) {
                const float s0 = __builtin_fmaf(blo(kk[e][j + 3]), w00, __builtin_fmaf(blo(kk[e][j + 2]), w01, __builtin_fmaf(blo(kk[e][j + 1]), w02, blo(kk[e][j]) * w03)));
                const float s1 = __builtin_fmaf(bhi(kk[e][j + 3]), w10, __builtin_fmaf(bhi(kk[e][j + 2]), w11, __builtin_fmaf(bhi(kk[e][j + 1]), w12, bhi(kk[e][j]) * w13)));
                a0[j] = s0 * blo(vv[e][j]);
                a1[j] = s1 * bhi(vv[e][j]);
            }
            kvp[e] = half_xpose(ns(pkbf(a0[0], a0[1])), ns(pkbf(a0[2], a0[3])), ns(pkbf(a1[0], a1[1])), ns(pkbf(a1[2], a1[3])));
            if (e == 0) lwait(smem + C_K1, 4);
            st16(ws, o_w, s_w + e * 262144u, kvp[e]);
        }
    }
    STAMP(1);
    SETPRIO(XPRIO);
    lsig(sbase + C_ROW);
    if (Q_POS == 1) q_loads();
    int io = (r & 1) == 0 ? 128 * (lg >> 1) + 4 * (lg & 1) + 8 * li : 256 + 64 * lg + (lg < 2 ? 4 : 2) * li;
    int st2 = (r & 1) && lg >= 2 ? 32 : 2;
    pin(io); pin(st2);
    lwait(smem + C_K, 8);
    STAMP(2);
    {
        const char* bb = smem + L_SCR + kch * SCR_CH + (41 + li) * SCR_ROW + lg * 16;
        const char* ab = smem + L_SCR + kch * SCR_CH + ((r & 1) * 16 + li) * SCR_ROW + lg * 16;
        const float ysc = *(const float*)(smem + L_YSC + kch * 4);
        v4f c0 = {0.f, 0.f, 0.f, 0.f}, c1 = c0;
#pragma unroll
        for (int ks = 0; ks < 8; ++ks) {
            const v8h bf = *(const v8h*)(bb + ks * 64);
            c0 = __builtin_amdgcn_mfma_f32_16x16x32_f16(*(const v8h*)(ab + ks * 64), bf, c0, 0, 0, 0);
            c1 = __builtin_amdgcn_mfma_f32_16x16x32_f16(*(const v8h*)(bb - 9 * SCR_ROW + ks * 64), bf, c1, 0, 0, 0);
        }
        SGB(0x100, 10);
#pragma unroll
        for (int ks = 0; ks < 5; ++ks) { SGB(0x8, 2); SGB(0x100, 3); }
        SGB(0x8, 6);
        char* tab = smem + L_KTAB + kch * 1536;
        asm volatile("s_nop 7\n\ts_nop 2" : "+a"(c0), "+a"(c1));
        *(u32*)(tab + io * 2) = pkbf(c0[0] * ysc, c0[1] * ysc);
        *(u32*)(tab + (io + st2) * 2) = pkbf(c0[2] * ysc, c0[3] * ysc);
        const u32 p01 = pkbf(c1[0] * ysc, c1[1] * ysc), p23 = pkbf(c1[2] * ysc, c1[3] * ysc);
        u16* t2 = (u16*)tab + 512 + 64 * lg + li;
        t2[0] = (u16)p01; t2[16] = (u16)(p01 >> 16); t2[32] = (u16)p23; t2[48] = (u16)(p23 >> 16);
    }
    lsig(sbase + C_ROW);
    STAMP(3);
    const u32 o_fl = WS_FLAG + (u32)((cid >> 6) * 256 + (t & 255)) * 8u;
    u32 x[8], yd[8];
    u64 fl;
    Stat s;
    float ga[8], gb[8], ca[8], cb[8];
    {
#pragma unroll
        for (int e = 0; e < 2; ++e) {
            const float d0 = blo(Dch[e]);
            u32 o[4];
            to_rowlay((v4u){pkbf(blo(kvp[e].x) * d0, bhi(kvp[e].x) * d0), pkbf(blo(kvp[e].y) * d0, bhi(kvp[e].y) * d0),
                            pkbf(blo(kvp[e].z) * d0, bhi(kvp[e].z) * d0), pkbf(blo(kvp[e].w) * d0, bhi(kvp[e].w) * d0)}, o);
#pragma unroll
            for (int k = 0; k < 4; ++k) yd[e * 4 + k] = o[k];
        }
#pragma unroll
        for (int k = 0; k < 8; ++k) pin(yd[k]);
#pragma unroll
        for (int e = 0; e < 2; ++e) {
            u32 o[4];
            to_rowlay(half_xpose(__builtin_amdgcn_perm(qq[e][1], qq[e][0], 0x05040100u), __builtin_amdgcn_perm(qq[e][3], qq[e][2], 0x05040100u),
                                 __builtin_amdgcn_perm(qq[e][1], qq[e][0], 0x07060302u), __builtin_amdgcn_perm(qq[e][3], qq[e][2], 0x07060302u)), o);
#pragma unroll
            for (int k = 0; k < 4; ++k) x[e * 4 + k] = o[k];
        }
        s.ma = blo(pil); s.mb = bhi(pil);
        float da[8], db[8], na, nb;
#pragma unroll
        for (int k = 0; k < 8; ++k) { da[k] = blo(x[k]) - s.ma; db[k] = bhi(x[k]) - s.mb; }
        row_stats(smem + L_RR, r, lane, da, db, s, na, nb);
#pragma unroll
        for (int k = 0; k < 8; ++k) {
            const float ea = blo(x[k]) - s.ma, eb = bhi(x[k]) - s.mb;
            const u32 pd = pkbf(ea, eb);
            const u32 n0 = pkbf(blo(pd) * s.ra, bhi(pd) * s.rb);
            ga[k] = blo(n0); gb[k] = bhi(n0);
            ca[k] = __builtin_fmaf(ga[k], blo(yd[k]), ea);
            cb[k] = __builtin_fmaf(gb[k], bhi(yd[k]), eb);
            pin(ca[k]); pin(cb[k]);
        }
    }
    STAMP(7);
    fl = ldf(ws, o_fl, 8192);
    auto taps = [&]() {
    lwait(smem + C_ROW, 8);
    {
        const int c = 255 - (t & 255);
        v4u kc[2];
        if (r < 2) {
#pragma unroll
            for (int e = 0; e < 2; ++e) {
                const char* tab = smem + L_KTAB + e * 1536;
                const u32 a = *(const u16*)(tab + (384 + c) * 2), bq = *(const u16*)(tab + (385 + c) * 2);
                const u32 aa = a | (a << 16), bb = bq | (bq << 16);
                kc[e] = (v4u){aa, aa, (c & 1) ? bb : (a | (bq << 16)), bb};
            }
        } else if (r == 2) {
#pragma unroll
            for (int e = 0; e < 2; ++e) {
                const char* tab = smem + L_KTAB + e * 1536;
                const u32 d0 = *(const u32*)(tab + (256 + 2 * c) * 2), d1 = *(const u32*)(tab + (258 + 2 * c) * 2);
                kc[e] = (v4u){__builtin_amdgcn_perm(d0, d0, 0x01000100u), d0, __builtin_amdgcn_perm(d0, d0, 0x03020302u), __builtin_amdgcn_perm(d1, d1, 0x01000100u)};
            }
        } else {
            const bool s1 = c >= 32;
#pragma unroll
            for (int e = 0; e < 2; ++e) {
                const char* a = smem + L_KTAB + e * 1536 + (s1 ? (128 + 4 * c) * 2 : 16 * c);
                const v2u d01 = *(const v2u*)a, d23 = *(const v2u*)(a + 8);
                kc[e].x = s1 ? __builtin_amdgcn_perm(d01.x, d01.x, 0x01000100u) : d01.x;
                kc[e].y = s1 ? __builtin_amdgcn_alignbit(d01.y, d01.x, 16) : d01.y;
                kc[e].z = s1 ? __builtin_amdgcn_perm(d01.y, d01.y, 0x01000100u) : d23.x;
                kc[e].w = s1 ? __builtin_amdgcn_alignbit(d23.x, d01.y, 16) : d23.y;
            }
        }
        *(v4u*)(smem + L_KL + (c + 1) * 16) = kc[0];
        *(v4u*)(smem + L_KL + KL_CH + (c + 1) * 16) = kc[1];
    }
    lsig(sbase + C_ROW);
    };
    const bool late = lpeek(smem + (half ? C_Y : C_CL), 4 + 4 * half);
    if (!late) taps();
    STAMP(4);
    TAG();
    pollf(ws, o_fl, 8192, fl, e1_, dead);
    STAMP(5);
    SETPRIO(3);
    const u32 o_yr = (u32)lane * 128u;
    const u32 s_yr = WS_Y + (u32)(rid >> 3) * 65536u + (u32)(rid & 7) * 16u + (u32)r * 16384u;
    u32 y[8];
    float da[8], db[8], na, nb;
    if (!lpeek(smem + C_Y, 4 * half + 4)) {
        lwait(smem + C_Y, 4 * half + 4);
        __builtin_amdgcn_s_sleep(DLY_Y);
    }
    STAMP(8);
    get_y(ws, o_yr, s_yr, s_yr + (YL ? 4096u : 8192u), y, dead);
    const u32 c_m1 = sbase + (half ? C_RY : C_DUM), c_m2 = sbase + (half ? C_DUM : C_RY);
    lsig(c_m1);
    lwait(smem + C_CL, 4);
    STAMP(9);
#pragma unroll
    for (int k = 0; k < 8; ++k) {
        da[k] = __builtin_fmaf(ga[k], blo(y[k]), ca[k]);
        db[k] = __builtin_fmaf(gb[k], bhi(y[k]), cb[k]);
    }
    row_stats(smem + L_RR + 256, r, lane, da, db, s, na, nb);
    put_u(ws, o_w, s_w + MB2, da, db, s, na, nb);
    STAMP(11);
    if (late) taps();
    SETPRIO(0);
#pragma unroll
    for (int k = 0; k < 8; ++k) {
        const u32 z = add2(y[k], yd[k]);
        x[k] = add2(x[k], pkbf(ga[k] * blo(z), gb[k] * bhi(z)));
        ga[k] = blo(z); gb[k] = bhi(z);
        const float ea = blo(x[k]) - s.ma, eb = bhi(x[k]) - s.mb;
        const u32 pd = pkbf(ea, eb);
        const u32 u = pkbf(blo(pd) * s.ra, bhi(pd) * s.rb);
        const float dk = DSK(k);
        yd[k] = pkbf(blo(u) * dk, bhi(u) * dk);
        ca[k] = ea + blo(yd[k]);
        cb[k] = eb + bhi(yd[k]);
        pin(ca[k]); pin(cb[k]); pin(ga[k]); pin(gb[k]);
    }
    wait_vm0();
    lsig(c_m2);
    if (b == 0 && t == 256) st8(ws, WS_EPOCH, 0, tag);
    SETPRIO(3);
    STAMP(12);
    if (!lpeek(smem + C_Y, 4 * half + 12)) {
        lwait(smem + C_Y, 4 * half + 12);
        __builtin_amdgcn_s_sleep(DLY_Y);
    }
    STAMP(15);
    get_y(ws, o_yr, s_yr + MB2, s_yr + MB2 + (YL ? 4096u : 8192u), y, dead);
    lsig(c_m1);
    lwait(smem + C_CL, 8);
    STAMP(16);
#pragma unroll
    for (int k = 0; k < 8; ++k) { da[k] = blo(y[k]) + ca[k]; db[k] = bhi(y[k]) + cb[k]; }
    Stat s2 = s;
    row_stats(smem + L_RR + 512, r, lane, da, db, s2, na, nb);
#pragma unroll
    for (int k = 0; k < 8; ++k) {
        da[k] = __builtin_fmaf(__builtin_fmaf(da[k], s2.ra, na), ga[k], da[k]);
        db[k] = __builtin_fmaf(__builtin_fmaf(db[k], s2.rb, nb), gb[k], db[k]);
    }
    STAMP(17);
    row_stats(smem + L_RR + 768, r, lane, da, db, s, na, nb);
    put_u(ws, o_w, s_w + 2 * MB2, da, db, s, na, nb);
    STAMP(18);
    SETPRIO(0);
    {
        char* tr = smem + L_SCR + r * 4352;
#pragma unroll
        for (int k = 0; k < 8; ++k) {
            u32 xx = add2(x[k], add2(y[k], yd[k]));
            const u32 n = ln2(xx, s2);
            xx = add2(xx, pkbf(blo(n) * ga[k], bhi(n) * gb[k]));
            const u32 u = ln2(xx, s);
            const float dk = DSK(k);
            const u32 d = pkbf(blo(u) * dk, bhi(u) * dk);
            const int co = (64 * (k >> 2) + 32 * (k & 1) + ((k >> 1) & 1)) * 4;
            *(float*)(tr + lg * 1088 + li * 8 + co) = blo(xx) + blo(d);
            *(float*)(tr + lg * 1088 + 544 + li * 8 + co) = bhi(xx) + bhi(d);
        }
        wait_vm0();
        lsig(c_m2);
#pragma unroll
        for (int e = 0; e < 2; ++e) {
            const v4f c0 = *(const v4f*)(tr + lg * 1088 + li * 32 + e * 16), c1 = *(const v4f*)(tr + lg * 1088 + 544 + li * 32 + e * 16);
#pragma unroll
            for (int k = 0; k < 4; ++k) { ca[e * 4 + k] = c0[k]; cb[e * 4 + k] = c1[k]; pin(ca[e * 4 + k]); pin(cb[e * 4 + k]); }
        }
    }
    SETPRIO(3);
    STAMP(19);
    if (!lpeek(smem + C_Y, 4 * half + 20)) {
        lwait(smem + C_Y, 4 * half + 20);
        __builtin_amdgcn_s_sleep(DLY_Y);
    }
    STAMP(22);
    get_y(ws, (u32)(li & 8) * 1024u + (u32)(li & 7) * 512u + (u32)(lg >> 1) * 128u + (u32)(lg & 1) * 4096u, s_yr + 2 * MB2, s_yr + 2 * MB2 + 256u, y, dead);
    STAMP(23);
    {
        const rsrc_t ro = mkr(p.out, SL * DM * 2);
        const u32 oo = (u32)lg * 2048u + (u32)li * 16u;
        v4u o0, o1;
#pragma unroll
        for (int k = 0; k < 4; ++k) {
            o0[k] = pkbf(blo(y[2 * k]) + ca[2 * k], blo(y[2 * k + 1]) + ca[2 * k + 1]);
            o1[k] = pkbf(bhi(y[2 * k]) + cb[2 * k], bhi(y[2 * k + 1]) + cb[2 * k + 1]);
        }
        st16(ro, oo, s_rw, o0);
        st16(ro, oo + 1024u, s_rw, o1);
    }
    STAMP(28);
}
#if defined(__HIP_DEVICE_COMPILE__)
#pragma clang attribute pop
#endif

extern "C" void kernel_launch(void* const* d_in, const int* in_sizes, int n_in,
                              void* d_out, int out_size, void* d_ws, size_t ws_size,
                              hipStream_t stream) {
    Params p;
    p.q = (const u16*)d_in[0];
    p.k = (const u16*)d_in[1];
    p.v = (const u16*)d_in[2];
    p.sk = (const u16*)d_in[3];
    p.ssmk = (const u16*)d_in[4];
    p.ssmD = (const u16*)d_in[5];
    p.logA = (const u16*)d_in[7];
    p.Aim = (const u16*)d_in[8];
    p.Cr = (const u16*)d_in[9];
    p.Ci = (const u16*)d_in[10];
    p.logdt = (const u16*)d_in[11];
    p.Dskip = (const u16*)d_in[12];
    p.out = (u16*)d_out;
    p.ws = (char*)d_ws;
    mega<<<256, 512, 0, stream>>>(p);
}
```

```cpp
#if defined(__HIP_DEVICE_COMPILE__)
#pragma clang attribute push(__attribute__((target("no-packed-fp32-ops"))), apply_to = function)
#endif
#include <hip/hip_runtime.h>
#include <stdint.h>

typedef unsigned short u16;
typedef unsigned u32;
typedef unsigned long long u64;
typedef u32 v4u __attribute__((ext_vector_type(4)));
typedef u32 v2u __attribute__((ext_vector_type(2)));
typedef float v4f __attribute__((ext_vector_type(4)));
typedef float v2f __attribute__((ext_vector_type(2)));
typedef __bf16 v2bf __attribute__((ext_vector_type(2)));
typedef __bf16 v8bf __attribute__((ext_vector_type(8)));
typedef _Float16 v8h __attribute__((ext_vector_type(8)));
typedef _Float16 v2h __attribute__((ext_vector_type(2)));
#define GAS __attribute__((address_space(1)))
#define LAS __attribute__((address_space(3)))
typedef GAS u64 gu64;
#define RLX_AGENT __ATOMIC_RELAXED, __HIP_MEMORY_SCOPE_AGENT

constexpr int SL = 2048, DM = 512, NST = 128;
constexpr u32 SENT = 0xFFFFFFFFu;
constexpr u32 SPIN_CAP = 1u << 15;

constexpr u32 WS_EPOCH = 0;
constexpr u32 WS_FLAG = 4096;
constexpr u32 MB1 = 1u << 20;
constexpr u32 MB2 = (2u << 20) + 65536u;
constexpr u32 WS_U = 65536u;
constexpr u32 WS_Y = WS_U + 3u * MB2;
constexpr u32 WS_BYTES = WS_U + 6u * MB2;
constexpr u32 LSPIN_CAP = 1u << 20;

constexpr int L_RR = 0;
constexpr int L_CNT = 1024;
constexpr int C_CONV = L_CNT, C_ROW = L_CNT + 64, C_Y = L_CNT + 128, C_K = L_CNT + 192, C_K1 = L_CNT + 224, L_YSC = L_CNT + 240, C_RY = L_CNT + 96, C_DUM = L_CNT + 160, C_CL = L_CNT + 32;
constexpr u32 LINC = 1u << 17;
constexpr int T_CH = 8 * 258 * 16;
constexpr int L_T = 1280;
constexpr int SCR_ROW = 528;
constexpr int SCR_CH = 57 * SCR_ROW;
constexpr int L_SCR = L_T + 2 * T_CH;
constexpr int U_CH = 320 * 16;
constexpr int L_U = L_SCR + 2 * SCR_CH;
constexpr int L_KTAB = L_U + 4 * U_CH;
constexpr int L_KL = L_KTAB + 2 * 1536;
constexpr int KL_CH = 257 * 16;
constexpr int L_TOTAL0 = L_KL + 2 * KL_CH;

constexpr int L_TOTAL = L_TOTAL0;
#define STAMP(i) do {} while (0)
#define STAMP_OUT() do {} while (0)
static_assert(L_TOTAL <= 163840, "lds");
struct Params {
    const u16 *q, *k, *v, *sk, *ssmk, *ssmD, *logA, *Aim, *Cr, *Ci, *logdt, *Dskip;
    u16* out;
    char* ws;
};

#define TLOAD_ASM \
    "ds_read_b128 a[0:3], %0 offset:4032\n\t" \
    "ds_read_b128 a[32:35], %0 offset:3520\n\t" \
    "ds_read_b128 a[64:67], %0 offset:3008\n\t" \
    "ds_read_b128 a[96:99], %0 offset:2496\n\t" \
    "ds_read_b128 a[4:7], %0 offset:3968\n\t" \
    "ds_read_b128 a[36:39], %0 offset:3456\n\t" \
    "ds_read_b128 a[68:71], %0 offset:2944\n\t" \
    "ds_read_b128 a[100:103], %0 offset:2432\n\t" \
    "ds_read_b128 a[8:11], %0 offset:3904\n\t" \
    "ds_read_b128 a[40:43], %0 offset:3392\n\t" \
    "ds_read_b128 a[72:75], %0 offset:2880\n\t" \
    "ds_read_b128 a[104:107], %0 offset:2368\n\t" \
    "ds_read_b128 a[12:15], %0 offset:3840\n\t" \
    "ds_read_b128 a[44:47], %0 offset:3328\n\t" \
    "ds_read_b128 a[76:79], %0 offset:2816\n\t" \
    "ds_read_b128 a[108:111], %0 offset:2304\n\t" \
    "ds_read_b128 a[16:19], %0 offset:3776\n\t" \
    "ds_read_b128 a[48:51], %0 offset:3264\n\t" \
    "ds_read_b128 a[80:83], %0 offset:2752\n\t" \
    "ds_read_b128 a[112:115], %0 offset:2240\n\t" \
    "ds_read_b128 a[20:23], %0 offset:3712\n\t" \
    "ds_read_b128 a[52:55], %0 offset:3200\n\t" \
    "ds_read_b128 a[84:87], %0 offset:2688\n\t" \
    "ds_read_b128 a[116:119], %0 offset:2176\n\t" \
    "ds_read_b128 a[24:27], %0 offset:3648\n\t" \
    "ds_read_b128 a[56:59], %0 offset:3136\n\t" \
    "ds_read_b128 a[88:91], %0 offset:2624\n\t" \
    "ds_read_b128 a[120:123], %0 offset:2112\n\t" \
    "ds_read_b128 a[28:31], %0 offset:3584\n\t" \
    "ds_read_b128 a[60:63], %0 offset:3072\n\t" \
    "ds_read_b128 a[92:95], %0 offset:2560\n\t" \
    "ds_read_b128 a[124:127], %0 offset:2048\n\t"
#define TLOAD_CLOB "a0", "a1", "a2", "a3", "a4", "a5", "a6", "a7", "a8", "a9", "a10", "a11", "a12", "a13", "a14", "a15", "a16", "a17", "a18", "a19", "a20", "a21", "a22", "a23", "a24", "a25", "a26", "a27", "a28", "a29", "a30", "a31", "a32", "a33", "a34", "a35", "a36", "a37", "a38", "a39", "a40", "a41", "a42", "a43", "a44", "a45", "a46", "a47", "a48", "a49", "a50", "a51", "a52", "a53", "a54", "a55", "a56", "a57", "a58", "a59", "a60", "a61", "a62", "a63", "a64", "a65", "a66", "a67", "a68", "a69", "a70", "a71", "a72", "a73", "a74", "a75", "a76", "a77", "a78", "a79", "a80", "a81", "a82", "a83", "a84", "a85", "a86", "a87", "a88", "a89", "a90", "a91", "a92", "a93", "a94", "a95", "a96", "a97", "a98", "a99", "a100", "a101", "a102", "a103", "a104", "a105", "a106", "a107", "a108", "a109", "a110", "a111", "a112", "a113", "a114", "a115", "a116", "a117", "a118", "a119", "a120", "a121", "a122", "a123", "a124", "a125", "a126", "a127"
#define CONV_CLOB "v64", "v65", "v66", "v67", "v68", "v69", "v70", "v71", "v72", "v73", "v74", "v75", "v76", "v77", "v78", "v79", "v80", "v81", "v82", "v83", "v84", "v85", "v86", "v87", "v88", "v89", "v90", "v91", "v92", "v93", "v94", "v95", "v96", "v97", "v98", "v99", "v100", "v101", "v102", "v103", "v104", "v105", "v106", "v107", "v108", "v109", "v110", "v111", "v112", "v113", "v114", "v115", "v116", "v117", "v118", "v119", "v120", "v121", "v122", "v123", "v124", "v125", "v126", "v127"
#define TRI_ASM \
    "1:\n\t" \
    "s_sub_u32 %4, %4, 1\n\t" \
    "ds_read_b32 v112, %6\n\t" \
    "ds_read_b128 v[64:67], %7 offset:1984\n\t" \
    "ds_read_b128 v[68:71], %7 offset:1472\n\t" \
    "ds_read_b128 v[72:75], %7 offset:960\n\t" \
    "ds_read_b128 v[76:79], %7 offset:448\n\t" \
    "ds_read_b128 v[80:83], %7 offset:1920\n\t" \
    "ds_read_b128 v[84:87], %7 offset:1408\n\t" \
    "ds_read_b128 v[88:91], %7 offset:896\n\t" \
    "ds_read_b128 v[92:95], %7 offset:384\n\t" \
    "ds_read_b128 v[96:99], %7 offset:1856\n\t" \
    "ds_read_b128 v[100:103], %7 offset:1344\n\t" \
    "ds_read_b128 v[104:107], %7 offset:832\n\t" \
    "ds_read_b128 v[108:111], %7 offset:320\n\t" \
    "s_waitcnt lgkmcnt(12)\n\t" \
    "v_readfirstlane_b32 %5, v112\n\t" \
    "s_cmp_lt_u32 %5, %4\n\t" \
    "s_cbranch_scc1 1b\n\t" \
    "s_waitcnt lgkmcnt(11)\n\t" \
    "v_mfma_f32_16x16x32_bf16 %2, a[0:3], v[64:67], %2\n\t" \
    "ds_read_b128 v[112:115], %7 offset:1792\n\t" \
    "s_waitcnt lgkmcnt(11)\n\t" \
    "v_mfma_f32_16x16x32_bf16 %3, a[32:35], v[68:71], %3\n\t" \
    "ds_read_b128 v[116:119], %7 offset:1280\n\t" \
    "s_waitcnt lgkmcnt(11)\n\t" \
    "v_mfma_f32_16x16x32_bf16 %0, a[0:3], v[72:75], %0\n\t" \
    "ds_read_b128 v[120:123], %7 offset:768\n\t" \
    "v_mfma_f32_16x16x32_bf16 %2, a[64:67], v[72:75], %2\n\t" \
    "ds_read_b128 v[124:127], %7 offset:256\n\t" \
    "s_waitcnt lgkmcnt(12)\n\t" \
    "v_mfma_f32_16x16x32_bf16 %1, a[32:35], v[76:79], %1\n\t" \
    "v_mfma_f32_16x16x32_bf16 %3, a[96:99], v[76:79], %3\n\t" \
    "s_waitcnt lgkmcnt(10)\n\t" \
    "v_mfma_f32_16x16x32_bf16 %2, a[4:7], v[80:83], %2\n\t" \
    "ds_read_b128 v[64:67], %7 offset:1728\n\t" \
    "v_mfma_f32_16x16x32_bf16 %3, a[36:39], v[84:87], %3\n\t" \
    "ds_read_b128 v[68:71], %7 offset:1216\n\t" \
    "s_waitcnt lgkmcnt(10)\n\t" \
    "v_mfma_f32_16x16x32_bf16 %0, a[4:7], v[88:91], %0\n\t" \
    "ds_read_b128 v[72:75], %7 offset:704\n\t" \
    "v_mfma_f32_16x16x32_bf16 %2, a[68:71], v[88:91], %2\n\t" \
    "ds_read_b128 v[76:79], %7 offset:192\n\t" \
    "v_mfma_f32_16x16x32_bf16 %1, a[36:39], v[92:95], %1\n\t" \
    "v_mfma_f32_16x16x32_bf16 %3, a[100:103], v[92:95], %3\n\t" \
    "s_waitcnt lgkmcnt(10)\n\t" \
    "v_mfma_f32_16x16x32_bf16 %2, a[8:11], v[96:99], %2\n\t" \
    "ds_read_b128 v[80:83], %7 offset:1664\n\t" \
    "v_mfma_f32_16x16x32_bf16 %3, a[40:43], v[100:103], %3\n\t" \
    "ds_read_b128 v[84:87], %7 offset:1152\n\t" \
    "s_waitcnt lgkmcnt(10)\n\t" \
    "v_mfma_f32_16x16x32_bf16 %0, a[8:11], v[104:107], %0\n\t" \
    "ds_read_b128 v[88:91], %7 offset:640\n\t" \
    "v_mfma_f32_16x16x32_bf16 %2, a[72:75], v[104:107], %2\n\t" \
    "ds_read_b128 v[92:95], %7 offset:128\n\t" \
    "v_mfma_f32_16x16x32_bf16 %1, a[40:43], v[108:111], %1\n\t" \
    "v_mfma_f32_16x16x32_bf16 %3, a[104:107], v[108:111], %3\n\t" \
    "s_waitcnt lgkmcnt(10)\n\t" \
    "v_mfma_f32_16x16x32_bf16 %2, a[12:15], v[112:115], %2\n\t" \
    "ds_read_b128 v[96:99], %7 offset:1600\n\t" \
    "v_mfma_f32_16x16x32_bf16 %3, a[44:47], v[116:119], %3\n\t" \
    "ds_read_b128 v[100:103], %7 offset:1088\n\t" \
    "s_waitcnt lgkmcnt(10)\n\t" \
    "v_mfma_f32_16x16x32_bf16 %0, a[12:15], v[120:123], %0\n\t" \
    "ds_read_b128 v[104:107], %7 offset:576\n\t" \
    "v_mfma_f32_16x16x32_bf16 %2, a[76:79], v[120:123], %2\n\t" \
    "ds_read_b128 v[108:111], %7 offset:64\n\t" \
    "v_mfma_f32_16x16x32_bf16 %1, a[44:47], v[124:127], %1\n\t" \
    "v_mfma_f32_16x16x32_bf16 %3, a[108:111], v[124:127], %3\n\t" \
    "s_waitcnt lgkmcnt(10)\n\t" \
    "v_mfma_f32_16x16x32_bf16 %2, a[16:19], v[64:67], %2\n\t" \
    "ds_read_b128 v[112:115], %7 offset:1536\n\t" \
    "v_mfma_f32_16x16x32_bf16 %3, a[48:51], v[68:71], %3\n\t" \
    "ds_read_b128 v[116:119], %7 offset:1024\n\t" \
    "s_waitcnt lgkmcnt(10)\n\t" \
    "v_mfma_f32_16x16x32_bf16 %0, a[16:19], v[72:75], %0\n\t" \
    "ds_read_b128 v[120:123], %7 offset:512\n\t" \
    "v_mfma_f32_16x16x32_bf16 %2, a[80:83], v[72:75], %2\n\t" \
    "ds_read_b128 v[124:127], %7 offset:0\n\t" \
    "v_mfma_f32_16x16x32_bf16 %1, a[48:51], v[76:79], %1\n\t" \
    "v_mfma_f32_16x16x32_bf16 %3, a[112:115], v[76:79], %3\n\t" \
    "s_waitcnt lgkmcnt(10)\n\t" \
    "v_mfma_f32_16x16x32_bf16 %2, a[20:23], v[80:83], %2\n\t" \
    "v_mfma_f32_16x16x32_bf16 %3, a[52:55], v[84:87], %3\n\t" \
    "s_waitcnt lgkmcnt(8)\n\t" \
    "v_mfma_f32_16x16x32_bf16 %0, a[20:23], v[88:91], %0\n\t" \
    "v_mfma_f32_16x16x32_bf16 %2, a[84:87], v[88:91], %2\n\t" \
    "v_mfma_f32_16x16x32_bf16 %1, a[52:55], v[92:95], %1\n\t" \
    "v_mfma_f32_16x16x32_bf16 %3, a[116:119], v[92:95], %3\n\t" \
    "s_waitcnt lgkmcnt(6)\n\t" \
    "v_mfma_f32_16x16x32_bf16 %2, a[24:27], v[96:99], %2\n\t" \
    "v_mfma_f32_16x16x32_bf16 %3, a[56:59], v[100:103], %3\n\t" \
    "s_waitcnt lgkmcnt(4)\n\t" \
    "v_mfma_f32_16x16x32_bf16 %0, a[24:27], v[104:107], %0\n\t" \
    "v_mfma_f32_16x16x32_bf16 %2, a[88:91], v[104:107], %2\n\t" \
    "v_mfma_f32_16x16x32_bf16 %1, a[56:59], v[108:111], %1\n\t" \
    "v_mfma_f32_16x16x32_bf16 %3, a[120:123], v[108:111], %3\n\t" \
    "s_waitcnt lgkmcnt(2)\n\t" \
    "v_mfma_f32_16x16x32_bf16 %2, a[28:31], v[112:115], %2\n\t" \
    "v_mfma_f32_16x16x32_bf16 %3, a[60:63], v[116:119], %3\n\t" \
    "s_waitcnt lgkmcnt(0)\n\t" \
    "v_mfma_f32_16x16x32_bf16 %0, a[28:31], v[120:123], %0\n\t" \
    "v_mfma_f32_16x16x32_bf16 %2, a[92:95], v[120:123], %2\n\t" \
    "v_mfma_f32_16x16x32_bf16 %1, a[60:63], v[124:127], %1\n\t" \
    "v_mfma_f32_16x16x32_bf16 %3, a[124:127], v[124:127], %3\n\t" \
    "s_nop 7\n\t" \
    "s_nop 1\n\t"
#define CROSS_ASM \
    "ds_read_b128 v[64:67], %5 offset:1984\n\t" \
    "ds_read_b128 v[68:71], %5 offset:1472\n\t" \
    "ds_read_b128 v[72:75], %6 offset:1984\n\t" \
    "ds_read_b128 v[76:79], %6 offset:1472\n\t" \
    "ds_read_b128 v[80:83], %5 offset:960\n\t" \
    "ds_read_b128 v[84:87], %5 offset:448\n\t" \
    "ds_read_b128 v[88:91], %6 offset:960\n\t" \
    "ds_read_b128 v[92:95], %6 offset:448\n\t" \
    "ds_read_b128 v[96:99], %5 offset:1920\n\t" \
    "ds_read_b128 v[100:103], %5 offset:1408\n\t" \
    "ds_read_b128 v[104:107], %6 offset:1920\n\t" \
    "ds_read_b128 v[108:111], %6 offset:1408\n\t" \
    "ds_read_b128 v[112:115], %5 offset:896\n\t" \
    "s_waitcnt lgkmcnt(11)\n\t" \
    "v_mfma_f32_16x16x32_bf16 %0, a[64:67], v[64:67], %0\n\t" \
    "ds_read_b128 v[116:119], %5 offset:384\n\t" \
    "v_mfma_f32_16x16x32_bf16 %1, a[96:99], v[68:71], %1\n\t" \
    "ds_read_b128 v[120:123], %6 offset:896\n\t" \
    "s_waitcnt lgkmcnt(11)\n\t" \
    "v_mfma_f32_16x16x32_bf16 %2, v[72:75], v[64:67], %2\n\t" \
    "ds_read_b128 v[124:127], %6 offset:384\n\t" \
    "v_mfma_f32_16x16x32_bf16 %3, v[76:79], v[68:71], %3\n\t" \
    "ds_read_b128 v[64:67], %5 offset:1856\n\t" \
    "s_waitcnt lgkmcnt(11)\n\t" \
    "v_mfma_f32_16x16x32_bf16 %0, v[72:75], v[80:83], %0\n\t" \
    "ds_read_b128 v[68:71], %5 offset:1344\n\t" \
    "v_mfma_f32_16x16x32_bf16 %1, v[76:79], v[84:87], %1\n\t" \
    "ds_read_b128 v[72:75], %6 offset:1856\n\t" \
    "s_waitcnt lgkmcnt(11)\n\t" \
    "v_mfma_f32_16x16x32_bf16 %2, v[88:91], v[80:83], %2\n\t" \
    "ds_read_b128 v[76:79], %6 offset:1344\n\t" \
    "v_mfma_f32_16x16x32_bf16 %3, v[92:95], v[84:87], %3\n\t" \
    "ds_read_b128 v[80:83], %5 offset:832\n\t" \
    "s_waitcnt lgkmcnt(11)\n\t" \
    "v_mfma_f32_16x16x32_bf16 %0, a[68:71], v[96:99], %0\n\t" \
    "ds_read_b128 v[84:87], %5 offset:320\n\t" \
    "v_mfma_f32_16x16x32_bf16 %1, a[100:103], v[100:103], %1\n\t" \
    "ds_read_b128 v[88:91], %6 offset:832\n\t" \
    "s_waitcnt lgkmcnt(11)\n\t" \
    "v_mfma_f32_16x16x32_bf16 %2, v[104:107], v[96:99], %2\n\t" \
    "ds_read_b128 v[92:95], %6 offset:320\n\t" \
    "v_mfma_f32_16x16x32_bf16 %3, v[108:111], v[100:103], %3\n\t" \
    "ds_read_b128 v[96:99], %5 offset:1792\n\t" \
    "s_waitcnt lgkmcnt(11)\n\t" \
    "v_mfma_f32_16x16x32_bf16 %0, v[104:107], v[112:115], %0\n\t" \
    "ds_read_b128 v[100:103], %5 offset:1280\n\t" \
    "v_mfma_f32_16x16x32_bf16 %1, v[108:111], v[116:119], %1\n\t" \
    "ds_read_b128 v[104:107], %6 offset:1792\n\t" \
    "s_waitcnt lgkmcnt(11)\n\t" \
    "v_mfma_f32_16x16x32_bf16 %2, v[120:123], v[112:115], %2\n\t" \
    "ds_read_b128 v[108:111], %6 offset:1280\n\t" \
    "v_mfma_f32_16x16x32_bf16 %3, v[124:127], v[116:119], %3\n\t" \
    "ds_read_b128 v[112:115], %5 offset:768\n\t" \
    "s_waitcnt lgkmcnt(11)\n\t" \
    "v_mfma_f32_16x16x32_bf16 %0, a[72:75], v[64:67], %0\n\t" \
    "ds_read_b128 v[116:119], %5 offset:256\n\t" \
    "v_mfma_f32_16x16x32_bf16 %1, a[104:107], v[68:71], %1\n\t" \
    "ds_read_b128 v[120:123], %6 offset:768\n\t" \
    "s_waitcnt lgkmcnt(11)\n\t" \
    "v_mfma_f32_16x16x32_bf16 %2, v[72:75], v[64:67], %2\n\t" \
    "ds_read_b128 v[124:127], %6 offset:256\n\t" \
    "v_mfma_f32_16x16x32_bf16 %3, v[76:79], v[68:71], %3\n\t" \
    "ds_read_b128 v[64:67], %5 offset:1728\n\t" \
    "s_waitcnt lgkmcnt(11)\n\t" \
    "v_mfma_f32_16x16x32_bf16 %0, v[72:75], v[80:83], %0\n\t" \
    "buffer_load_dwordx4 %4, %7, %8, %9 offen sc1\n\t" \
    "ds_read_b128 v[68:71], %5 offset:1216\n\t" \
    "v_mfma_f32_16x16x32_bf16 %1, v[76:79], v[84:87], %1\n\t" \
    "ds_read_b128 v[72:75], %6 offset:1728\n\t" \
    "s_waitcnt lgkmcnt(11)\n\t" \
    "v_mfma_f32_16x16x32_bf16 %2, v[88:91], v[80:83], %2\n\t" \
    "ds_read_b128 v[76:79], %6 offset:1216\n\t" \
    "v_mfma_f32_16x16x32_bf16 %3, v[92:95], v[84:87], %3\n\t" \
    "ds_read_b128 v[80:83], %5 offset:704\n\t" \
    "s_waitcnt lgkmcnt(11)\n\t" \
    "v_mfma_f32_16x16x32_bf16 %0, a[76:79], v[96:99], %0\n\t" \
    "ds_read_b128 v[84:87], %5 offset:192\n\t" \
    "v_mfma_f32_16x16x32_bf16 %1, a[108:111], v[100:103], %1\n\t" \
    "ds_read_b128 v[88:91], %6 offset:704\n\t" \
    "s_waitcnt lgkmcnt(11)\n\t" \
    "v_mfma_f32_16x16x32_bf16 %2, v[104:107], v[96:99], %2\n\t" \
    "ds_read_b128 v[92:95], %6 offset:192\n\t" \
    "v_mfma_f32_16x16x32_bf16 %3, v[108:111], v[100:103], %3\n\t" \
    "ds_read_b128 v[96:99], %5 offset:1664\n\t" \
    "s_waitcnt lgkmcnt(11)\n\t" \
    "v_mfma_f32_16x16x32_bf16 %0, v[104:107], v[112:115], %0\n\t" \
    "ds_read_b128 v[100:103], %5 offset:1152\n\t" \
    "v_mfma_f32_16x16x32_bf16 %1, v[108:111], v[116:119], %1\n\t" \
    "ds_read_b128 v[104:107], %6 offset:1664\n\t" \
    "s_waitcnt lgkmcnt(11)\n\t" \
    "v_mfma_f32_16x16x32_bf16 %2, v[120:123], v[112:115], %2\n\t" \
    "ds_read_b128 v[108:111], %6 offset:1152\n\t" \
    "v_mfma_f32_16x16x32_bf16 %3, v[124:127], v[116:119], %3\n\t" \
    "ds_read_b128 v[112:115], %5 offset:640\n\t" \
    "s_waitcnt lgkmcnt(11)\n\t" \
    "v_mfma_f32_16x16x32_bf16 %0, a[80:83], v[64:67], %0\n\t" \
    "ds_read_b128 v[116:119], %5 offset:128\n\t" \
    "v_mfma_f32_16x16x32_bf16 %1, a[112:115], v[68:71], %1\n\t" \
    "ds_read_b128 v[120:123], %6 offset:640\n\t" \
    "s_waitcnt lgkmcnt(11)\n\t" \
    "v_mfma_f32_16x16x32_bf16 %2, v[72:75], v[64:67], %2\n\t" \
    "ds_read_b128 v[124:127], %6 offset:128\n\t" \
    "v_mfma_f32_16x16x32_bf16 %3, v[76:79], v[68:71], %3\n\t" \
    "ds_read_b128 v[64:67], %5 offset:1600\n\t" \
    "s_waitcnt lgkmcnt(11)\n\t" \
    "v_mfma_f32_16x16x32_bf16 %0, v[72:75], v[80:83], %0\n\t" \
    "ds_read_b128 v[68:71], %5 offset:1088\n\t" \
    "v_mfma_f32_16x16x32_bf16 %1, v[76:79], v[84:87], %1\n\t" \
    "ds_read_b128 v[72:75], %6 offset:1600\n\t" \
    "s_waitcnt lgkmcnt(11)\n\t" \
    "v_mfma_f32_16x16x32_bf16 %2, v[88:91], v[80:83], %2\n\t" \
    "ds_read_b128 v[76:79], %6 offset:1088\n\t" \
    "v_mfma_f32_16x16x32_bf16 %3, v[92:95], v[84:87], %3\n\t" \
    "ds_read_b128 v[80:83], %5 offset:576\n\t" \
    "s_waitcnt lgkmcnt(11)\n\t" \
    "v_mfma_f32_16x16x32_bf16 %0, a[84:87], v[96:99], %0\n\t" \
    "ds_read_b128 v[84:87], %5 offset:64\n\t" \
    "v_mfma_f32_16x16x32_bf16 %1, a[116:119], v[100:103], %1\n\t" \
    "ds_read_b128 v[88:91], %6 offset:576\n\t" \
    "s_waitcnt lgkmcnt(11)\n\t" \
    "v_mfma_f32_16x16x32_bf16 %2, v[104:107], v[96:99], %2\n\t" \
    "ds_read_b128 v[92:95], %6 offset:64\n\t" \
    "v_mfma_f32_16x16x32_bf16 %3, v[108:111], v[100:103], %3\n\t" \
    "ds_read_b128 v[96:99], %5 offset:1536\n\t" \
    "s_waitcnt lgkmcnt(11)\n\t" \
    "v_mfma_f32_16x16x32_bf16 %0, v[104:107], v[112:115], %0\n\t" \
    "ds_read_b128 v[100:103], %5 offset:1024\n\t" \
    "v_mfma_f32_16x16x32_bf16 %1, v[108:111], v[116:119], %1\n\t" \
    "ds_read_b128 v[104:107], %6 offset:1536\n\t" \
    "s_waitcnt lgkmcnt(11)\n\t" \
    "v_mfma_f32_16x16x32_bf16 %2, v[120:123], v[112:115], %2\n\t" \
    "ds_read_b128 v[108:111], %6 offset:1024\n\t" \
    "v_mfma_f32_16x16x32_bf16 %3, v[124:127], v[116:119], %3\n\t" \
    "ds_read_b128 v[112:115], %5 offset:512\n\t" \
    "s_waitcnt lgkmcnt(11)\n\t" \
    "v_mfma_f32_16x16x32_bf16 %0, a[88:91], v[64:67], %0\n\t" \
    "ds_read_b128 v[116:119], %5 offset:0\n\t" \
    "v_mfma_f32_16x16x32_bf16 %1, a[120:123], v[68:71], %1\n\t" \
    "ds_read_b128 v[120:123], %6 offset:512\n\t" \
    "s_waitcnt lgkmcnt(11)\n\t" \
    "v_mfma_f32_16x16x32_bf16 %2, v[72:75], v[64:67], %2\n\t" \
    "ds_read_b128 v[124:127], %6 offset:0\n\t" \
    "v_mfma_f32_16x16x32_bf16 %3, v[76:79], v[68:71], %3\n\t" \
    "s_waitcnt lgkmcnt(10)\n\t" \
    "v_mfma_f32_16x16x32_bf16 %0, v[72:75], v[80:83], %0\n\t" \
    "v_mfma_f32_16x16x32_bf16 %1, v[76:79], v[84:87], %1\n\t" \
    "s_waitcnt lgkmcnt(8)\n\t" \
    "v_mfma_f32_16x16x32_bf16 %2, v[88:91], v[80:83], %2\n\t" \
    "v_mfma_f32_16x16x32_bf16 %3, v[92:95], v[84:87], %3\n\t" \
    "s_waitcnt lgkmcnt(6)\n\t" \
    "v_mfma_f32_16x16x32_bf16 %0, a[92:95], v[96:99], %0\n\t" \
    "v_mfma_f32_16x16x32_bf16 %1, a[124:127], v[100:103], %1\n\t" \
    "s_waitcnt lgkmcnt(4)\n\t" \
    "v_mfma_f32_16x16x32_bf16 %2, v[104:107], v[96:99], %2\n\t" \
    "v_mfma_f32_16x16x32_bf16 %3, v[108:111], v[100:103], %3\n\t" \
    "s_waitcnt lgkmcnt(2)\n\t" \
    "v_mfma_f32_16x16x32_bf16 %0, v[104:107], v[112:115], %0\n\t" \
    "v_mfma_f32_16x16x32_bf16 %1, v[108:111], v[116:119], %1\n\t" \
    "s_waitcnt lgkmcnt(0)\n\t" \
    "v_mfma_f32_16x16x32_bf16 %2, v[120:123], v[112:115], %2\n\t" \
    "v_mfma_f32_16x16x32_bf16 %3, v[124:127], v[116:119], %3\n\t" \
    "s_nop 7\n\t" \
    "s_nop 1\n\t" \
    "s_waitcnt vmcnt(0)\n\t"

__device__ __forceinline__ float u2f(u32 x) { return __builtin_bit_cast(float, x); }
__device__ __forceinline__ u32 f2u(float x) { return __builtin_bit_cast(u32, x); }
__device__ __forceinline__ float blo(u32 x) { return u2f(x << 16); }
__device__ __forceinline__ float bhi(u32 x) { return u2f(x & 0xffff0000u); }
__device__ __forceinline__ u32 pkbf(float a, float b) {
    v2f f = {a, b};
    return __builtin_bit_cast(u32, __builtin_convertvector(f, v2bf));
}
__device__ __forceinline__ float rbf(float a) { return blo(pkbf(a, a)); }
__device__ __forceinline__ u32 pkh(float a, float b) {
    v2f f = {a, b};
    return __builtin_bit_cast(u32, __builtin_convertvector(f, v2h));
}
template <int CTRL> __device__ __forceinline__ float dppf(float x) {
    return __builtin_bit_cast(float, __builtin_amdgcn_mov_dpp(__builtin_bit_cast(int, x), CTRL, 0xf, 0xf, true));
}
template <int CTRL> __device__ __forceinline__ u32 dppu(u32 x) {
    return (u32)__builtin_amdgcn_mov_dpp((int)x, CTRL, 0xf, 0xf, true);
}
constexpr int XOR1 = 0xB1, XOR2 = 0x4E, HMIR = 0x141, MIR = 0x140;
__device__ __forceinline__ void bar() { asm volatile("s_waitcnt lgkmcnt(0)\n\ts_barrier" ::: "memory"); }
__device__ __forceinline__ void wait_vm0() { asm volatile("s_waitcnt vmcnt(0)" ::: "memory"); }
__device__ __forceinline__ u32 umin(u32 a, u32 b) { return a < b ? a : b; }
__device__ __forceinline__ u32 umax(u32 a, u32 b) { return a > b ? a : b; }
__device__ __forceinline__ u32 ns(u32 x) { return umin(x, SENT - 1u); }
__device__ __forceinline__ bool isvalid(v4u x) { return umax(umax(x.x, x.y), umax(x.z, x.w)) != SENT; }
template <class T> __device__ __forceinline__ void pin(T& x) { asm volatile("" : "+v"(x)); }

typedef __amdgpu_buffer_rsrc_t rsrc_t;
__device__ __forceinline__ rsrc_t mkr(const void* p, u32 bytes) { return __builtin_amdgcn_make_buffer_rsrc((void*)p, 0, bytes, 0x00020000); }
__device__ __forceinline__ void st16(rsrc_t r, u32 off, u32 so, v4u v) {
    asm volatile("" : "+v"(v));
    __builtin_amdgcn_raw_buffer_store_b128(v, r, off, so, 16);
    asm volatile("s_nop 1" :: "v"(v));
}
__device__ __forceinline__ void st8(rsrc_t r, u32 off, u32 so, v2u v) { __builtin_amdgcn_raw_buffer_store_b64(v, r, off, so, 16); }
__device__ __forceinline__ v4u ld16(rsrc_t r, u32 off, u32 so) { return __builtin_amdgcn_raw_buffer_load_b128(r, off, so, 16); }
__device__ __forceinline__ u32 ldw(rsrc_t r, u32 off, u32 so) { return __builtin_amdgcn_raw_buffer_load_b32(r, off, so, 0); }
__device__ __forceinline__ u32 ldh(rsrc_t r, u32 off, u32 so) { return (u32)__builtin_amdgcn_raw_buffer_load_b16(r, off, so, 0); }

__device__ __forceinline__ void wait_flag(rsrc_t r, u32 off, v2u tag, bool& dead) {
    u32 spins = 0;
    while (!dead) {
        asm volatile("" ::: "memory");
        const v2u x = __builtin_amdgcn_raw_buffer_load_b64(r, off, 0, 16);
        if (x.x == tag.x && x.y == tag.y) break;
        if (++spins > SPIN_CAP) dead = true;
    }
}
__device__ __forceinline__ void repoll16(rsrc_t r, u32 off, u32 so, v4u& x, bool& dead) {
    u32 spins = 0;
    while (!isvalid(x) && !dead) {
        asm volatile("" ::: "memory");
        x = ld16(r, off, so);
        if (++spins > SPIN_CAP) dead = true;
    }
}

__device__ __forceinline__ void build_T(char* base, v4u lo, v4u hi) {
    const u32 W[8] = {lo.x, lo.y, lo.z, lo.w, hi.x, hi.y, hi.z, hi.w};
    u32 R[8], B[8];
#pragma unroll
    for (int m = 1; m < 8; ++m) {
        R[m] = __builtin_amdgcn_alignbit(W[m], W[m], 16);
        B[m] = (W[m] & 0xffffu) | (W[m - 1] & 0xffff0000u);
    }
#pragma unroll
    for (int rho = 0; rho < 8; ++rho) {
        const int idx = rho < 4 ? 2 * rho : 2 * (rho - 4) + 1;
        v4u o;
        if (rho & 1) { const int m = (7 + rho) / 2; o = (v4u){R[m], R[m - 1], R[m - 2], R[m - 3]}; }
        else { const int m = 4 + rho / 2; o = (v4u){B[m], B[m - 1], B[m - 2], B[m - 3]}; }
        *(v4u*)(base + idx * 258 * 16) = o;
    }
}

__device__ __forceinline__ void to_rowlay(v4u a, u32 (&o)[4]) {
    auto s0 = __builtin_amdgcn_permlane32_swap(a.x, a.z, false, false);
    auto s1 = __builtin_amdgcn_permlane32_swap(a.y, a.w, false, false);
    auto s2 = __builtin_amdgcn_permlane16_swap(s0[0], s1[0], false, false);
    auto s3 = __builtin_amdgcn_permlane16_swap(s0[1], s1[1], false, false);
    o[0] = s2[0]; o[1] = s2[1]; o[2] = s3[0]; o[3] = s3[1];
}
__device__ __forceinline__ v4u from_rowlay(const u32 (&o)[4]) {
    auto s2 = __builtin_amdgcn_permlane16_swap(o[0], o[1], false, false);
    auto s3 = __builtin_amdgcn_permlane16_swap(o[2], o[3], false, false);
    auto s0 = __builtin_amdgcn_permlane32_swap(s2[0], s3[0], false, false);
    auto s1 = __builtin_amdgcn_permlane32_swap(s2[1], s3[1], false, false);
    return (v4u){s0[0], s1[0], s0[1], s1[1]};
}

__device__ __forceinline__ void lsig(u32 addr) {
    asm volatile("s_mov_b64 exec, 1\n\tds_add_u32 %0, %1\n\ts_mov_b64 exec, -1" :: "v"(addr), "v"(LINC) : "memory");
}
#define SETPRIO(x) __builtin_amdgcn_s_setprio(x)
__device__ __forceinline__ void lwait(const char* p, u32 target) {
    u32 tg = target * LINC + 1u;
    do {
        asm volatile("" ::: "memory");
        --tg;
    } while ((u32)__builtin_amdgcn_readfirstlane(*(const u32*)p) < tg);
    asm volatile("" ::: "memory");
}
__device__ __forceinline__ bool lpeek(const char* p, u32 target) {
    asm volatile("" ::: "memory");
    return (u32)__builtin_amdgcn_readfirstlane(*(const u32*)p) >= target * LINC;
}
#define POLL_HEAD "s_mov_b64 vcc, -1\n\t1:\n\ts_mov_b64 exec, vcc\n\t"
#define POLL_MID "s_mov_b64 exec, -1\n\ts_sub_i32 %0, %0, 1\n\ts_cmp_gt_i32 %0, 0\n\ts_cselect_b64 %1, -1, 0\n\t"
#define POLL_TAIL "s_and_b64 vcc, vcc, %1\n\ts_cbranch_scc1 1b\n\t"
__device__ __forceinline__ void poll2(rsrc_t ws, u32 off, u32 so0, u32 so1, v4u& a, v4u& b, int& bud) {
    u64 m; u32 t;
    asm volatile(POLL_HEAD
        "buffer_load_dwordx4 v[56:59], %5, %6, %7 offen sc1\n\t"
        "buffer_load_dwordx4 v[60:63], %5, %6, %8 offen sc1\n\t"
        POLL_MID
        "s_waitcnt vmcnt(1)\n\t"
        "v_max3_u32 %2, v56, v57, v58\n\t"
        "s_waitcnt vmcnt(0)\n\t"
        "v_max3_u32 %2, v59, v60, %2\n\t"
        "v_max3_u32 %2, v61, v62, %2\n\t"
        "v_max_u32 %2, v63, %2\n\t"
        "v_cmp_eq_u32 vcc, -1, %2\n\t"
        POLL_TAIL
        : "+s"(bud), "=&s"(m), "=&v"(t), "=&{v[56:59]}"(a), "=&{v[60:63]}"(b) : "v"(off), "s"(ws), "s"(so0), "s"(so1) : "memory", "vcc", "scc");
}
__device__ __forceinline__ void poll1(rsrc_t ws, u32 off, u32 so, v4u& a, int& bud) {
    u64 m; u32 t;
    asm volatile(POLL_HEAD
        "buffer_load_dwordx4 v[56:59], %4, %5, %6 offen sc1\n\t"
        POLL_MID
        "s_waitcnt vmcnt(0)\n\t"
        "v_max3_u32 %2, v56, v57, v58\n\t"
        "v_max_u32 %2, v59, %2\n\t"
        "v_cmp_eq_u32 vcc, -1, %2\n\t"
        POLL_TAIL
        : "+s"(bud), "=&s"(m), "=&v"(t), "=&{v[56:59]}"(a) : "v"(off), "s"(ws), "s"(so) : "memory", "vcc", "scc");
}
__device__ __forceinline__ void pollf(rsrc_t ws, u32 off, u32 so, u64 f, u64 tag, int& bud) {
    if (__builtin_amdgcn_ballot_w64(f != tag) != 0) {
        u64 m;
        asm volatile(POLL_HEAD
            "buffer_load_dwordx2 %2, %3, %4, %5 offen sc1\n\t"
            POLL_MID
            "s_waitcnt vmcnt(0)\n\t"
            "v_cmp_ne_u64 vcc, %6, %2\n\t"
            POLL_TAIL
            : "+s"(bud), "=&s"(m), "=&v"(f) : "v"(off), "s"(ws), "s"(so), "s"(tag) : "memory", "vcc", "scc");
    }
}
__device__ __forceinline__ u64 ldf(rsrc_t ws, u32 off, u32 so) { return __builtin_bit_cast(u64, __builtin_amdgcn_raw_buffer_load_b64(ws, off, so, 16)); }

struct Stat { float ma, mb, ra, rb; };
__device__ __forceinline__ void row_stats(char* rr, int r, int lane, const float (&da)[8], const float (&db)[8], Stat& s, float& na, float& nb) {
    float v0 = ((da[0] + da[1]) + (da[2] + da[3])) + ((da[4] + da[5]) + (da[6] + da[7]));
    float v2 = ((db[0] + db[1]) + (db[2] + db[3])) + ((db[4] + db[5]) + (db[6] + db[7]));
    float v1 = da[0] * da[0], v1b = da[1] * da[1], v3 = db[0] * db[0], v3b = db[1] * db[1];
#pragma unroll
    for (int k = 2; k < 8; k += 2) {
        v1 = __builtin_fmaf(da[k], da[k], v1); v1b = __builtin_fmaf(da[k + 1], da[k + 1], v1b);
        v3 = __builtin_fmaf(db[k], db[k], v3); v3b = __builtin_fmaf(db[k + 1], db[k + 1], v3b);
    }
    v1 += v1b; v3 += v3b;
    asm volatile(
        "s_nop 1\n\t"
        "v_add_f32_dpp %0, %0, %0 row_ror:8 row_mask:0xf bank_mask:0x3\n\t"
        "v_add_f32_dpp %1, %1, %1 row_ror:8 row_mask:0xf bank_mask:0x3\n\t"
        "v_add_f32_dpp %0, %2, %2 row_ror:8 row_mask:0xf bank_mask:0xc\n\t"
        "v_add_f32_dpp %1, %3, %3 row_ror:8 row_mask:0xf bank_mask:0xc\n\t"
        "s_nop 0\n\t"
        "v_add_f32_dpp %0, %0, %0 row_half_mirror row_mask:0xf bank_mask:0x5\n\t"
        "v_add_f32_dpp %0, %1, %1 row_half_mirror row_mask:0xf bank_mask:0xa\n\t"
        "s_nop 1\n\t"
        "v_add_f32_dpp %0, %0, %0 quad_perm:[1,0,3,2] row_mask:0xf bank_mask:0xf\n\t"
        "s_nop 1\n\t"
        "v_add_f32_dpp %0, %0, %0 quad_perm:[2,3,0,1] row_mask:0xf bank_mask:0xf\n\t"
        : "+v"(v0), "+v"(v1) : "v"(v2), "v"(v3));
    *(u32*)(rr + (lane >> 4) * 64 + r * 16 + (lane & 12)) = ns(f2u(v0));
    v4u zi;
    u32 spins = 0;
    do {
        asm volatile("" ::: "memory");
        zi = *(const v4u*)(rr + (lane >> 4) * 64 + (lane & 3) * 16);
    } while (__builtin_amdgcn_ballot_w64(!isvalid(zi)) != 0 && ++spins < LSPIN_CAP);
    v4f z = __builtin_bit_cast(v4f, zi);
#pragma unroll
    for (int k = 0; k < 4; ++k) z[k] += dppf<XOR1>(z[k]);
#pragma unroll
    for (int k = 0; k < 4; ++k) z[k] += dppf<XOR2>(z[k]);
    const float inv = 1.f / DM;
    const float mua = z[0] * inv, mub = z[2] * inv;
    const float va = __builtin_fmaxf(__builtin_fmaf(-mua, mua, z[1] * inv), 0.f), vb = __builtin_fmaxf(__builtin_fmaf(-mub, mub, z[3] * inv), 0.f);
    const u32 pv = pkbf(va, vb);
    const u32 pe = pkbf(blo(pv) + 1e-5f, bhi(pv) + 1e-5f);
    const u32 pd = pkbf(__builtin_amdgcn_sqrtf(blo(pe)), __builtin_amdgcn_sqrtf(bhi(pe)));
    const u32 pm = pkbf(s.ma + mua, s.mb + mub);
    s.ra = __builtin_amdgcn_rcpf(blo(pd));
    s.rb = __builtin_amdgcn_rcpf(bhi(pd));
    na = (s.ma - blo(pm)) * s.ra;
    nb = (s.mb - bhi(pm)) * s.rb;
    s.ma = blo(pm);
    s.mb = bhi(pm);
}
#define MFMA(a, b, c) __builtin_amdgcn_mfma_f32_16x16x32_bf16(a, b, c, 0, 0, 0)
#define SGB(m, n) __builtin_amdgcn_sched_group_barrier(m, n, 0)
__device__ __forceinline__ u32 ln2(u32 x, const Stat& s) {
    const u32 pd = pkbf(blo(x) - s.ma, bhi(x) - s.mb);
    return pkbf(blo(pd) * s.ra, bhi(pd) * s.rb);
}
__device__ __forceinline__ u32 add2(u32 a, u32 b) { return pkbf(blo(a) + blo(b), bhi(a) + bhi(b)); }
__device__ __forceinline__ u32 mul2(u32 a, u32 b) { return pkbf(blo(a) * blo(b), bhi(a) * bhi(b)); }
__device__ __forceinline__ v4u pair_xpose(bool odd, u32 c0a, u32 c0b, u32 c1a, u32 c1b) {
    const u32 sa = odd ? c0a : c1a, sb = odd ? c0b : c1b;
    const u32 ra = dppu<XOR1>(sa), rb = dppu<XOR1>(sb);
    return (v4u){odd ? ra : c0a, odd ? rb : c0b, odd ? c1a : ra, odd ? c1b : rb};
}

struct cf { float r, i; };
__device__ __forceinline__ cf cmul(cf a, cf b) { return {__builtin_fmaf(a.r, b.r, -(a.i * b.i)), __builtin_fmaf(a.r, b.i, a.i * b.r)}; }
template <int K> __device__ __forceinline__ cf cexpk(float la, float th, float tl) {
    const float sc = (float)(1 << K);
    const float mag = __builtin_amdgcn_exp2f(la * sc);
    const float ph = __builtin_fmaf(tl, sc, __builtin_amdgcn_fractf(th * sc));
    return {mag * __builtin_amdgcn_cosf(ph), mag * __builtin_amdgcn_sinf(ph)};
}
__device__ __forceinline__ float exp_acc(float x) {
    const float y = x * 1.44269504088896340736f;
    const float n = __builtin_rintf(y);
    float f = __builtin_fmaf(x, 1.44269504088896340736f, -n);
    f = __builtin_fmaf(x, 1.92596299e-8f, f);
    const float nc = __builtin_fminf(__builtin_fmaxf(n, -300.f), 300.f);
    const float e = __builtin_ldexpf(__builtin_amdgcn_exp2f(f), (int)nc);
    return __builtin_fabsf(y) < 400.f ? e : __builtin_amdgcn_exp2f(y);
}

#define DLY_U0 210
#define DLY_U1 300
#define DLY_Y 20
#define DLY_K 6
#define DLY_S 4
#define SSMK_POS 0
#define Q_POS 0
#define YC_POS 1
#define CPRIO 1
#define RPRIO 0
#define XPRIO 0
#define ORDER 1
#define PRE_ADV 95
#define NOROW 0
#define YL 0
#define CPF 4
#define CODE_BYTES 23936
struct KG { float la, th, tl, dt; };
struct KP { u32 a, im, dt; };
__device__ __forceinline__ KP kg_load(const Params& p, int chan, int kn) {
    KP k;
    const u32 so = (u32)chan * (NST * 2);
    k.a = ldh(mkr(p.logA, DM * NST * 2), kn * 2, so);
    k.dt = ldh(mkr(p.logdt, DM * 2), 0, chan * 2);
    k.im = ldh(mkr(p.Aim, DM * NST * 2), kn * 2, so);
    return k;
}
__device__ __forceinline__ KG kg_prep(const KP& k) {
    KG g;
    g.dt = rbf(exp_acc(blo(k.dt)));
    const float zr = -rbf(exp_acc(blo(k.a))) * g.dt;
    const float zi = blo(k.im) * g.dt;
    g.la = zr * 1.44269504088896340736f;
    g.th = zi * 0.15915494309189533577f;
    g.tl = __builtin_fmaf(zi, 6.4206383e-9f, __builtin_fmaf(zi, 0.15915494309189533577f, -g.th));
    return g;
}
#define FENCE() asm volatile("" ::: "memory")
#define PUTH(row, h) do { const cf h_ = (h); *(u32*)(scr + (row) * SCR_ROW) = pkh(h_.r, -h_.i); } while (0)
#define PUTL(row, l) do { const cf l_ = (l); *(u32*)(scr + (41 + (row)) * SCR_ROW) = pkh(l_.r, l_.i); } while (0)
#define CEXP(k) cexpk<k>(g.la, g.th, g.tl)
#define PUTP(row, v) (*(u32*)(scr + (row) * SCR_ROW) = (v))
__device__ __forceinline__ u32 pkc(cf a) { return pkh(a.r, -a.i); }
__device__ __forceinline__ u32 pkp(cf a) { return pkh(a.r, a.i); }
__device__ __forceinline__ u32 hmul(u32 x, u32 y) {
    u32 t;
    asm("v_pk_mul_f16 %0, %1, %2 op_sel:[0,0] op_sel_hi:[0,1]\n\t"
        "v_pk_fma_f16 %0, %1, %2, %0 op_sel:[1,1,0] op_sel_hi:[1,0,1] neg_lo:[0,1,0]" : "=&v"(t) : "v"(x), "v"(y));
    return t;
}
#define DSK(k) (((k) & 2) ? bhi(Dsk[((k) >> 2) * 2 + ((k) & 1)]) : blo(Dsk[((k) >> 2) * 2 + ((k) & 1)]))
__device__ __forceinline__ v4u half_xpose(u32 c0a, u32 c0b, u32 c1a, u32 c1b) {
    auto sa = __builtin_amdgcn_permlane32_swap(c0a, c1a, false, false);
    auto sb = __builtin_amdgcn_permlane32_swap(c0b, c1b, false, false);
    return (v4u){sa[0], sb[0], sa[1], sb[1]};
}

__device__ __forceinline__ void get_y(rsrc_t ws, u32 off, u32 so0, u32 so1, u32 (&y)[8], int& dead) {
    v4u y0, y1;
    poll2(ws, off, so0, so1, y0, y1, dead);
    u32 o[4];
    to_rowlay(y0, o);
#pragma unroll
    for (int k = 0; k < 4; ++k) y[k] = o[k];
    to_rowlay(y1, o);
#pragma unroll
    for (int k = 0; k < 4; ++k) y[4 + k] = o[k];
}
__device__ __forceinline__ void put_u(rsrc_t ws, u32 off, u32 so, const float (&da)[8], const float (&db)[8], const Stat& s, float na, float nb) {
#pragma unroll
    for (int e = 0; e < 2; ++e) {
        u32 uh[4];
#pragma unroll
        for (int k = 0; k < 4; ++k) uh[k] = ns(pkbf(__builtin_fmaf(da[e * 4 + k], s.ra, na), __builtin_fmaf(db[e * 4 + k], s.rb, nb)));
        st16(ws, off, so + e * 262144u, from_rowlay(uh));
    }
}

__global__ void __launch_bounds__(512, 2) mega(Params p) {
    __shared__ __attribute__((aligned(16))) char smem[L_TOTAL];
    const int t = __builtin_amdgcn_workitem_id_x(), lane = t & 63;
    u32 pf;
    {
        u64 pc0;
        const u32 po = umin((u32)(t >> 6) * 3072u + umin(lane, 23) * 128u, CODE_BYTES);
        asm volatile("s_getpc_b64 %0\n\tglobal_load_dword %1, %2, %0" : "=&s"(pc0), "=&v"(pf) : "v"(po));
    }
    const int w = __builtin_amdgcn_readfirstlane(t >> 6);
    const int b = __builtin_amdgcn_workgroup_id_x();
    const int cid = (b & 7) * 32 + (b >> 3);
    const int half = b >> 7;
    const int rid = half * 128 + (b & 7) * 16 + ((b >> 3) & 15);
    const rsrc_t ws = mkr(p.ws, WS_BYTES);
    int dead = SPIN_CAP;
    const v4u sent4 = {SENT, SENT, SENT, SENT};
#define CLR(o, so, lvl) st16(ws, o, so, sent4)
    const int li = lane & 15, lg = lane >> 4;
    const u32 sbase = (u32)(size_t)(LAS char*)smem;
    STAMP(0);
    asm volatile("" :: "s"(p.q), "s"(p.k), "s"(p.v), "s"(p.sk), "s"(p.ssmk), "s"(p.ssmD), "s"(p.logA), "s"(p.Aim), "s"(p.Cr), "s"(p.Ci), "s"(p.logdt), "s"(p.Dskip), "s"(p.out));
    u64 ep = *(const __attribute__((address_space(4))) u64*)(p.ws + WS_EPOCH);
#define BAR_EP() asm volatile("s_waitcnt lgkmcnt(0)\n\ts_barrier" : "+s"(ep) :: "memory")
    const int kch = (w >> 1) & 1, kn = (w & 1) * 64 + lane;
    char* scr = smem + L_SCR + kch * SCR_CH + kn * 4;
    const int slot = cid * 2 + kch;
    const int chan = (slot & ~63) + 2 * (slot & 31) + ((slot >> 5) & 1);
    const u32 o_w = (u32)lg * 65536u + (u32)li * 16u;
    const u32 s_w = WS_U + (u32)rid * 256u + (u32)(w & 3) * 524288u;
    const u32 o_yc = (u32)((lane >> 3) & 3) * 65536u + (u32)(lane >> 5) * 128u + (u32)(lane & 7) * 16u;
    const u32 s_yc = WS_Y + (u32)w * 262144u + (u32)cid * 256u;
    const u32 s_yw = WS_Y + (u32)(w & 1) * 131072u + (u32)slot * 128u;
#define TAG() u64 e1_ = ep; asm volatile("" : "+s"(e1_)); e1_ += 1ull; const v2u tag = {(u32)e1_, (u32)(e1_ >> 32)}
    const int r = w & 3;
    const u32 o_rw = (u32)(lane >> 5) * 4096u + (u32)(lane & 31) * 4u;
    const u32 s_rw = (u32)rid * 8192u + (u32)r * 256u;
    u32 kk[2][7], vv[2][4], Dch[2];
    v4u sk4[2];
    {
        const u32 oc = t < 256 ? o_w : 0x80000000u;
        CLR(oc, s_w, 3);
        CLR(oc, s_w + 262144u, 3);
    }
    KP kp;
    u32 pcr, pci, pcr2, pci2;
    {
        const u32 nb = w < 4 ? DM * NST * 2 : 0;
        asm volatile("buffer_load_ushort %0, %7, %9, %14 offen\n\tbuffer_load_ushort %1, off, %10, %15\n\tbuffer_load_ushort %2, %7, %11, %14 offen\n\t"
                     "buffer_load_ushort %3, %7, %12, %14 offen\n\tbuffer_load_ushort %4, %7, %13, %14 offen\n\t"
                     "buffer_load_ushort %5, %8, %12, %14 offen\n\tbuffer_load_ushort %6, %8, %13, %14 offen"
                     : "=&v"(kp.a), "=&v"(kp.dt), "=&v"(kp.im), "=&v"(pcr), "=&v"(pci), "=&v"(pcr2), "=&v"(pci2)
                     : "v"(kn * 2), "v"((kn ^ 64) * 2), "s"(mkr(p.logA, DM * NST * 2)), "s"(mkr(p.logdt, DM * 2)), "s"(mkr(p.Aim, DM * NST * 2)),
                       "s"(mkr(p.Cr, nb)), "s"(mkr(p.Ci, nb)), "s"(chan * (NST * 2)), "s"(chan * 2) : "memory");
    }
    FENCE();
    if (w >= 4) {
    {
        const rsrc_t rk = mkr(p.k, SL * DM * 2), rv = mkr(p.v, SL * DM * 2);
        const u32 ok = o_rw + s_rw - 3072u;
#pragma unroll
        for (int e = 0; e < 2; ++e) {
#pragma unroll
            for (int j = 0; j < 3; ++j) kk[e][j] = ldw(rk, ok + j * 1024u + e * 128u, 0);
#pragma unroll
            for (int j = 0; j < 4; ++j) kk[e][3 + j] = ldw(rk, o_rw + j * 1024u + e * 128u, s_rw);
#pragma unroll
            for (int j = 0; j < 4; ++j) vv[e][j] = ldw(rv, o_rw + j * 1024u + e * 128u, s_rw);
            sk4[e] = __builtin_amdgcn_raw_buffer_load_b128(mkr(p.sk, DM * 8), (lane & 31) * 16 + e * 512, r * 1024, 0);
            Dch[e] = ldh(mkr(p.ssmD, DM * 2), (lane & 31) * 4 + (lane >> 5) * 2 + e * 128, r * 256);
        }
    }
    }
    {
        u32 ia = L_U + (t >> 6) * U_CH + lane * 16;
        ia = t >= 256 ? L_RR + lane * 16 : ia;
        ia = t >= 320 ? L_CNT + li * 16 : ia;
        ia = t >= 384 ? L_T + (li >> 3) * T_CH + ((li & 7) * 258 + 257) * 16 : ia;
        ia = t >= 448 ? L_KL + (lane & 1) * KL_CH : ia;
        const u32 iv = (t >> 6) == 4 ? SENT : 0u;
        *(v4u*)(smem + ia) = (v4u){iv, iv, iv, iv};
    }
    STAMP(6);
    asm volatile("s_waitcnt lgkmcnt(0)\n\ts_barrier\n\ts_waitcnt vmcnt(26)\n\ts_cmp_lt_u32 %9, 4\n\ts_cbranch_scc0 1f\n\ts_waitcnt vmcnt(0)\n\t1:"
                 : "+s"(ep), "+v"(kp.a), "+v"(kp.dt), "+v"(kp.im), "+v"(pcr), "+v"(pci), "+v"(pcr2), "+v"(pci2), "+v"(pf) : "s"(w) : "memory", "scc");
    if (w < 4) {
        const int ch = w >> 1, par = w & 1;
        v4u sa0[2], sa1[2];
        auto late_loads = [&]() {
            FENCE();
        {
            const rsrc_t rs = mkr(p.ssmk + (size_t)chan * SL, SL * 2);
            const u32 o = (u32)(255 - (t & 127)) * 16u;
            sa1[0] = __builtin_amdgcn_raw_buffer_load_b128(rs, o, 0, 0);
            sa0[0] = __builtin_amdgcn_raw_buffer_load_b128(rs, o - 16u, 0, 0);
            sa1[1] = __builtin_amdgcn_raw_buffer_load_b128(rs, o - 2048u, 0, 0);
            sa0[1] = __builtin_amdgcn_raw_buffer_load_b128(rs, o - 2064u, 0, 0);
        }
        FENCE();
        };
        TAG();
        const u32 o_fs = lane == 0 ? WS_FLAG : 0x80000000u;
#define STFLAG(so) st8(ws, o_fs, so, tag)
        lsig(sbase + C_K1);
        STFLAG((w * 256 + cid) * 8);
        STAMP(7);
        const u32 pf1 = ldw(ws, (u32)lane * MB2 + (WS_U - 128u), 0);
        const u32 pf2 = ldw(mkr(p.out, SL * DM * 2), 0, rid * 8192);
        auto y0_clear = [&]() {
            FENCE();
            CLR(o_yc, s_yc, 2);
            CLR(o_yc, s_yc + MB1, 2);
            FENCE();
        };
        if (ORDER != 3) y0_clear();
        if (ORDER != 2 && ORDER != 4) late_loads();
        SETPRIO(CPRIO);
        {
            const KG g = kg_prep(kp);
            u32 cm = umax(umax(pcr & 0x7fffu, pci & 0x7fffu), umax(pcr2 & 0x7fffu, pci2 & 0x7fffu));
            cm = umax(cm, dppu<XOR1>(cm));
            cm = umax(cm, dppu<XOR2>(cm));
            cm = umax(cm, dppu<HMIR>(cm));
            cm = umax(cm, dppu<MIR>(cm));
            { auto sw = __builtin_amdgcn_permlane16_swap(cm, cm, false, false); cm = umax(sw[0], sw[1]); }
            { auto sw = __builtin_amdgcn_permlane32_swap(cm, cm, false, false); cm = umax(sw[0], sw[1]); }
            const int e = (int)(cm >> 7);
            const int sf = 267 - e < 253 ? 267 - e : 253;
            const float scale = u2f((u32)sf << 23);
            *(float*)(smem + L_YSC + kch * 4) = 2.f * g.dt * u2f((u32)(254 - sf) << 23);
            cf G[4], F[8];
            G[0] = {blo(pcr) * scale, blo(pci) * scale};
            const cf E7 = CEXP(7), E8 = CEXP(8), E9 = CEXP(9);
            G[1] = cmul(G[0], E7); G[2] = cmul(G[0], E8); G[3] = cmul(G[1], E8);
            F[1] = CEXP(0); F[2] = CEXP(1); F[4] = CEXP(2);
            F[3] = cmul(F[1], F[2]); F[5] = cmul(F[1], F[4]); F[6] = cmul(F[2], F[4]); F[7] = cmul(F[3], F[4]);
            u32 gp[4], fp[8];
#pragma unroll
            for (int a = 0; a < 4; ++a) gp[a] = pkc(G[a]);
#pragma unroll
            for (int a = 1; a < 8; ++a) fp[a] = pkc(F[a]);
            PUTP(0, gp[0]); PUTP(8, gp[1]); PUTP(16, gp[2]); PUTP(20, gp[3]);
            if (ORDER == 3) y0_clear();
            if (ORDER == 2) late_loads();
#pragma unroll
            for (int e2 = 1; e2 < 8; ++e2) { PUTP(e2, hmul(gp[0], fp[e2])); PUTP(8 + e2, hmul(gp[1], fp[e2])); }
#pragma unroll
            for (int e2 = 1; e2 < 4; ++e2) { PUTP(16 + e2, hmul(gp[2], fp[2 * e2])); PUTP(20 + e2, hmul(gp[3], fp[2 * e2])); }
            const u32 e9 = pkc(E9), e10 = pkc(CEXP(10));
#pragma unroll
            for (int a = 0; a < 4; ++a) {
                const u32 h = pkc(cmul(G[a], E9));
                PUTP(24 + 2 * a, h); PUTP(25 + 2 * a, hmul(h, fp[4])); PUTP(32 + a, hmul(h, e9)); PUTP(36 + a, hmul(h, e10));
            }
            PUTP(40, hmul(gp[0], pkc(CEXP(11))));
        }
        lsig(sbase + C_K);
        STAMP(1);
        const u32 o_fl1 = WS_FLAG + (u32)((cid >> 6) * 256 + (t >> 5) * 32 + (t & 1) * 16 + ((t >> 1) & 15)) * 8u;
        asm volatile("s_waitcnt vmcnt(4)" ::: "memory");
        if (ORDER != 3) STFLAG((w * 256 + cid) * 8 + 8192);
        asm volatile("" :: "v"(pf1), "v"(pf2));
        char* tslot = smem + L_T + ch * T_CH + ((t & 127) + 1) * 16;
        build_T(tslot, sa0[0], sa1[0]);
        FENCE();
        u64 fl = ldf(ws, o_fl1, 0);
        FENCE();
        build_T(tslot + 2048, sa0[1], sa1[1]);
        lsig(sbase + C_CONV);
        const int rho = li & 7;
        const u32 tb = sbase + L_T + ch * T_CH + ((rho < 4 ? 2 * rho : 2 * (rho - 4) + 1) * 258 + 2 - (li >> 3) + lg) * 16;
        const u32 ub = sbase + L_U + ch * U_CH + (4 * (li & 8) + 16 * par + 2 * (li & 7) + lg + 2) * 16;
        const u32 o_yw = (u32)(lg & 1) * 524288u + (u32)(li & 8) * 32768u + (u32)(li & 4) * 16384u + (u32)(li & 3) * 32u + (u32)(lg & 2) * 8u;
        const u32 o_ur = (u32)(t >> 1) * 256u + (u32)(t & 1) * 16u;
        const u32 s_ur = WS_U + (u32)(cid >> 3) * 65536u + (u32)(cid & 7) * 32u;
        char* uslot = smem + L_U + (t & 1) * U_CH + (64 + (t >> 1)) * 16;
        u32 tq = (u32)__builtin_amdgcn_s_memrealtime();
        STAMP(26);
        lwait(smem + C_CONV, 4);
        STAMP(2);
        asm volatile(TLOAD_ASM :: "v"(tb) : "memory", TLOAD_CLOB);
        pollf(ws, o_fl1, 0, fl, e1_, dead);
        if (NOROW) __builtin_amdgcn_s_sleep(NOROW);
        v4u uin;
        u32 tg = 8 * LINC + 1u;
#define PUT_Y(so) do { \
            a0 += a1; a2 += a3; \
            auto s0 = __builtin_amdgcn_permlane16_swap(ns(pkbf(a0[0], a0[1])), ns(pkbf(a2[0], a2[1])), false, false); \
            auto s1 = __builtin_amdgcn_permlane16_swap(ns(pkbf(a0[2], a0[3])), ns(pkbf(a2[2], a2[3])), false, false); \
            st16(ws, o_yw, so, (v4u){s0[0], s1[0], s0[1], s1[1]}); \
            lsig(sbase + C_Y); } while (0)
#define TRI(uu) do { u32 t1_ = tg, t2_; \
            asm volatile(TRI_ASM : "+v"(a0), "+v"(a1), "+v"(a2), "+v"(a3), "+s"(t1_), "=&s"(t2_) : "v"(sbase + C_CONV), "v"(uu) : "memory", "scc", CONV_CLOB); \
            tg += 4 * LINC; } while (0)
#pragma unroll 1
        for (int L = 0;; ++L) {
            const int uo = (L & 1) * 2 * U_CH;
            const u32 lo = (u32)L * MB2;
            const u32 u = ub + uo;
            if (L) STAMP(3 + L * 8);
            poll1(ws, o_ur, s_ur + lo, uin, dead);
            *(v4u*)(uslot + uo) = uin;
            lsig(sbase + C_CONV);
            u64 tn = __builtin_amdgcn_s_memrealtime();
            const u32 oob = L == 2 ? 0x80000000u : 0u;
            CLR(o_w | oob, s_w + lo + MB2, 1);
            CLR(o_w | oob, s_w + lo + MB2 + 262144u, 1);
            v4f a0 = {0.f, 0.f, 0.f, 0.f}, a1 = a0, a2 = a0, a3 = a0;
            STAMP(4 + L * 8);
            TRI(u);
            PUT_Y(s_yw + lo);
            STAMP(5 + L * 8);
            CLR(o_yc | oob, s_yc + lo + MB2, 1);
            CLR(o_yc | oob, s_yc + lo + MB2 + MB1, 1);
            a0 = (v4f){0.f, 0.f, 0.f, 0.f}; a1 = a0; a2 = a0; a3 = a0;
            asm volatile("" : "+s"(tn));
            const bool pre = (int)((u32)tn + PRE_ADV - tq) >= 0;
            asm volatile(CROSS_ASM : "+v"(a0), "+v"(a1), "+v"(a2), "+v"(a3), "=&v"(uin)
                         : "v"(u), "v"(tb), "v"(pre ? o_ur : 0x80000000u), "s"(ws), "s"(s_ur + lo + 32768u) : "memory", CONV_CLOB);
            lsig(sbase + C_CL);
            STAMP(6 + L * 8);
            if (!pre || __builtin_amdgcn_ballot_w64(!isvalid(uin)) != 0) {
                u32 n = 0;
#pragma nounroll
                while ((int)((u32)__builtin_amdgcn_s_memrealtime() - tq) < 0 && ++n < 1000) __builtin_amdgcn_s_sleep(1);
                poll1(ws, o_ur, s_ur + lo + 32768u, uin, dead);
            }
            *(v4u*)(uslot + uo + 2048) = uin;
            lsig(sbase + C_CONV);
            STFLAG((w * 256 + cid) * 8 + 16384 + L * 8192);
            STAMP(8 + L * 8);
            TRI(u + 2048);
            PUT_Y(s_yw + lo + MB1);
            STAMP(9 + L * 8);
            if (L == 2) break;
            tq = (u32)__builtin_amdgcn_s_memrealtime() + (L == 0 ? DLY_U0 : DLY_U1);
            if (L == 0) {
                lwait(smem + C_ROW, 12);
                STAMP(10);
                const char* kl = smem + L_KL + ch * KL_CH + (127 - (t & 127)) * 16;
                build_T(tslot, *(const v4u*)(kl + 2048), *(const v4u*)(kl + 2064));
                build_T(tslot + 2048, *(const v4u*)kl, *(const v4u*)(kl + 16));
                lsig(sbase + C_CONV);
                tg += 4 * LINC;
                lwait(smem + C_CONV, 16);
                asm volatile(TLOAD_ASM :: "v"(tb) : "memory", TLOAD_CLOB);
                STAMP(27);
            }
            fl = ldf(ws, o_fl1, 16384 + L * 8192);
            pollf(ws, o_fl1, 16384 + L * 8192, fl, e1_, dead);
            lwait(smem + C_RY, 4 * L + 4);
        }
        STAMP_OUT();
        return;
    }
    SETPRIO(RPRIO);
    {
        const KG g = kg_prep(kp);
        cf L[9];
        L[1] = CEXP(3); L[2] = CEXP(4); L[4] = CEXP(5); L[8] = CEXP(6);
        L[3] = cmul(L[1], L[2]); L[5] = cmul(L[1], L[4]); L[6] = cmul(L[2], L[4]); L[7] = cmul(L[3], L[4]);
        PUTP(41, 0x3c00u);
        const u32 l8 = pkp(L[8]);
#pragma unroll
        for (int i = 1; i < 8; ++i) { const u32 l = pkp(L[i]); PUTP(41 + i, l); PUTP(49 + i, hmul(l, l8)); }
        PUTP(49, l8);
    }
    lsig(sbase + C_K);
    asm volatile("" : "+v"(kk[0][0]), "+v"(kk[0][1]), "+v"(kk[0][2]), "+v"(kk[0][3]), "+v"(kk[0][4]), "+v"(kk[0][5]), "+v"(kk[0][6]), "+v"(sk4[0]) :: "memory");
    SETPRIO(3);
    STAMP(20);
    u32 qq[2][4], pil, Dsk[4];
    auto q_loads = [&]() {
    {
        const rsrc_t rq = mkr(p.q, SL * DM * 2);
#pragma unroll
        for (int e = 0; e < 2; ++e) {
#pragma unroll
            for (int j = 0; j < 4; ++j) qq[e][j] = ldw(rq, o_rw + j * 1024u + e * 128u, s_rw);
        }
        pil = ldh(rq, lg * 2048, rid * 8192) | (ldh(rq, lg * 2048 + 1024, rid * 8192) << 16);
    }
    {
        const rsrc_t rd2 = mkr(p.Dskip, DM * 2);
#pragma unroll
        for (int k = 0; k < 4; ++k) Dsk[k] = ldw(rd2, li * 4 + k * 64, r * 256);
    }
        FENCE();
    };
    if (Q_POS == 0) q_loads();
    FENCE();
    v4u kvp[2];
    {
#pragma unroll
        for (int e = 0; e < 2; ++e) {
            const float w00 = blo(sk4[e].x), w01 = bhi(sk4[e].x), w02 = blo(sk4[e].y), w03 = bhi(sk4[e].y);
            const float w10 = blo(sk4[e].z), w11 = bhi(sk4[e].z), w12 = blo(sk4[e].w), w13 = bhi(sk4[e].w);
            float a0[4], a1[4];
#pragma unroll
            for (int j = 0; j < 4; ++j) {
                const float s0 = __builtin_fmaf(blo(kk[e][j + 3]), w00, __builtin_fmaf(blo(kk[e][j + 2]), w01, __builtin_fmaf(blo(kk[e][j + 1]), w02, blo(kk[e][j]) * w03)));
                const float s1 = __builtin_fmaf(bhi(kk[e][j + 3]), w10, __builtin_fmaf(bhi(kk[e][j + 2]), w11, __builtin_fmaf(bhi(kk[e][j + 1]), w12, bhi(kk[e][j]) * w13)));
                a0[j] = s0 * blo(vv[e][j]);
                a1[j] = s1 * bhi(vv[e][j]);
            }
            kvp[e] = half_xpose(ns(pkbf(a0[0], a0[1])), ns(pkbf(a0[2], a0[3])), ns(pkbf(a1[0], a1[1])), ns(pkbf(a1[2], a1[3])));
            if (e == 0) lwait(smem + C_K1, 4);
            st16(ws, o_w, s_w + e * 262144u, kvp[e]);
        }
    }
    STAMP(1);
    SETPRIO(XPRIO);
    lsig(sbase + C_ROW);
    if (Q_POS == 1) q_loads();
    int io = (r & 1) == 0 ? 128 * (lg >> 1) + 4 * (lg & 1) + 8 * li : 256 + 64 * lg + (lg < 2 ? 4 : 2) * li;
    int st2 = (r & 1) && lg >= 2 ? 32 : 2;
    pin(io); pin(st2);
    lwait(smem + C_K, 8);
    STAMP(2);
    {
        const char* bb = smem + L_SCR + kch * SCR_CH + (41 + li) * SCR_ROW + lg * 16;
        const char* ab = smem + L_SCR + kch * SCR_CH + ((r & 1) * 16 + li) * SCR_ROW + lg * 16;
        const float ysc = *(const float*)(smem + L_YSC + kch * 4);
        v4f c0 = {0.f, 0.f, 0.f, 0.f}, c1 = c0;
#pragma unroll
        for (int ks = 0; ks < 8; ++ks) {
            const v8h bf = *(const v8h*)(bb + ks * 64);
            c0 = __builtin_amdgcn_mfma_f32_16x16x32_f16(*(const v8h*)(ab + ks * 64), bf, c0, 0, 0, 0);
            c1 = __builtin_amdgcn_mfma_f32_16x16x32_f16(*(const v8h*)(bb - 9 * SCR_ROW + ks * 64), bf, c1, 0, 0, 0);
        }
        SGB(0x100, 10);
#pragma unroll
        for (int ks = 0; ks < 5; ++ks) { SGB(0x8, 2); SGB(0x100, 3); }
        SGB(0x8, 6);
        char* tab = smem + L_KTAB + kch * 1536;
        asm volatile("s_nop 7\n\ts_nop 2" : "+a"(c0), "+a"(c1));
        *(u32*)(tab + io * 2) = pkbf(c0[0] * ysc, c0[1] * ysc);
        *(u32*)(tab + (io + st2) * 2) = pkbf(c0[2] * ysc, c0[3] * ysc);
        const u32 p01 = pkbf(c1[0] * ysc, c1[1] * ysc), p23 = pkbf(c1[2] * ysc, c1[3] * ysc);
        u16* t2 = (u16*)tab + 512 + 64 * lg + li;
        t2[0] = (u16)p01; t2[16] = (u16)(p01 >> 16); t2[32] = (u16)p23; t2[48] = (u16)(p23 >> 16);
    }
    lsig(sbase + C_ROW);
    STAMP(3);
    const u32 o_fl = WS_FLAG + (u32)((cid >> 6) * 256 + (t & 255)) * 8u;
    u32 x[8], yd[8];
    u64 fl;
    Stat s;
    float ga[8], gb[8], ca[8], cb[8];
    {
#pragma unroll
        for (int e = 0; e < 2; ++e) {
            pin(Dch[e]);
            const float d0 = blo(Dch[e]);
            u32 o[4];
            to_rowlay((v4u){pkbf(blo(kvp[e].x) * d0, bhi(kvp[e].x) * d0), pkbf(blo(kvp[e].y) * d0, bhi(kvp[e].y) * d0),
                            pkbf(blo(kvp[e].z) * d0, bhi(kvp[e].z) * d0), pkbf(blo(kvp[e].w) * d0, bhi(kvp[e].w) * d0)}, o);
#pragma unroll
            for (int k = 0; k < 4; ++k) yd[e * 4 + k] = o[k];
        }
#pragma unroll
        for (int k = 0; k < 8; ++k) pin(yd[k]);
#pragma unroll
        for (int e = 0; e < 2; ++e) {
            u32 o[4];
            to_rowlay(half_xpose(__builtin_amdgcn_perm(qq[e][1], qq[e][0], 0x05040100u), __builtin_amdgcn_perm(qq[e][3], qq[e][2], 0x05040100u),
                                 __builtin_amdgcn_perm(qq[e][1], qq[e][0], 0x07060302u), __builtin_amdgcn_perm(qq[e][3], qq[e][2], 0x07060302u)), o);
#pragma unroll
            for (int k = 0; k < 4; ++k) x[e * 4 + k] = o[k];
        }
        s.ma = blo(pil); s.mb = bhi(pil);
        float da[8], db[8], na, nb;
#pragma unroll
        for (int k = 0; k < 8; ++k) { da[k] = blo(x[k]) - s.ma; db[k] = bhi(x[k]) - s.mb; }
        row_stats(smem + L_RR, r, lane, da, db, s, na, nb);
#pragma unroll
        for (int k = 0; k < 8; ++k) {
            const float ea = blo(x[k]) - s.ma, eb = bhi(x[k]) - s.mb;
            const u32 pd = pkbf(ea, eb);
            const u32 n0 = pkbf(blo(pd) * s.ra, bhi(pd) * s.rb);
            ga[k] = blo(n0); gb[k] = bhi(n0);
            ca[k] = __builtin_fmaf(ga[k], blo(yd[k]), ea);
            cb[k] = __builtin_fmaf(gb[k], bhi(yd[k]), eb);
            pin(ca[k]); pin(cb[k]);
        }
    }
    STAMP(7);
    fl = ldf(ws, o_fl, 8192);
    auto taps = [&]() {
    lwait(smem + C_ROW, 8);
    {
        const int c = 255 - (t & 255);
        v4u kc[2];
        if (r < 2) {
#pragma unroll
            for (int e = 0; e < 2; ++e) {
                const char* tab = smem + L_KTAB + e * 1536;
                const u32 a = *(const u16*)(tab + (384 + c) * 2), bq = *(const u16*)(tab + (385 + c) * 2);
                const u32 aa = a | (a << 16), bb = bq | (bq << 16);
                kc[e] = (v4u){aa, aa, (c & 1) ? bb : (a | (bq << 16)), bb};
            }
        } else if (r == 2) {
#pragma unroll
            for (int e = 0; e < 2; ++e) {
                const char* tab = smem + L_KTAB + e * 1536;
                const u32 d0 = *(const u32*)(tab + (256 + 2 * c) * 2), d1 = *(const u32*)(tab + (258 + 2 * c) * 2);
                kc[e] = (v4u){__builtin_amdgcn_perm(d0, d0, 0x01000100u), d0, __builtin_amdgcn_perm(d0, d0, 0x03020302u), __builtin_amdgcn_perm(d1, d1, 0x01000100u)};
            }
        } else {
            const bool s1 = c >= 32;
#pragma unroll
            for (int e = 0; e < 2; ++e) {
                const char* a = smem + L_KTAB + e * 1536 + (s1 ? (128 + 4 * c) * 2 : 16 * c);
                const v2u d01 = *(const v2u*)a, d23 = *(const v2u*)(a + 8);
                kc[e].x = s1 ? __builtin_amdgcn_perm(d01.x, d01.x, 0x01000100u) : d01.x;
                kc[e].y = s1 ? __builtin_amdgcn_alignbit(d01.y, d01.x, 16) : d01.y;
                kc[e].z = s1 ? __builtin_amdgcn_perm(d01.y, d01.y, 0x01000100u) : d23.x;
                kc[e].w = s1 ? __builtin_amdgcn_alignbit(d23.x, d01.y, 16) : d23.y;
            }
        }
        *(v4u*)(smem + L_KL + (c + 1) * 16) = kc[0];
        *(v4u*)(smem + L_KL + KL_CH + (c + 1) * 16) = kc[1];
    }
    lsig(sbase + C_ROW);
    };
    const bool late = lpeek(smem + (half ? C_Y : C_CL), 4 + 4 * half);
    if (!late) taps();
    STAMP(4);
    TAG();
    pollf(ws, o_fl, 8192, fl, e1_, dead);
    STAMP(5);
    SETPRIO(3);
    const u32 o_yr = (u32)lane * 128u;
    const u32 s_yr = WS_Y + (u32)(rid >> 3) * 65536u + (u32)(rid & 7) * 16u + (u32)r * 16384u;
    u32 y[8];
    float da[8], db[8], na, nb;
    if (!lpeek(smem + C_Y, 4 * half + 4)) {
        lwait(smem + C_Y, 4 * half + 4);
        __builtin_amdgcn_s_sleep(DLY_Y);
    }
    STAMP(8);
    get_y(ws, o_yr, s_yr, s_yr + (YL ? 4096u : 8192u), y, dead);
    const u32 c_m1 = sbase + (half ? C_RY : C_DUM), c_m2 = sbase + (half ? C_DUM : C_RY);
    lsig(c_m1);
    lwait(smem + C_CL, 4);
    STAMP(9);
#pragma unroll
    for (int k = 0; k < 8; ++k) {
        da[k] = __builtin_fmaf(ga[k], blo(y[k]), ca[k]);
        db[k] = __builtin_fmaf(gb[k], bhi(y[k]), cb[k]);
    }
    row_stats(smem + L_RR + 256, r, lane, da, db, s, na, nb);
    put_u(ws, o_w, s_w + MB2, da, db, s, na, nb);
    STAMP(11);
    if (late) taps();
    SETPRIO(0);
#pragma unroll
    for (int k = 0; k < 8; ++k) {
        const u32 z = add2(y[k], yd[k]);
        x[k] = add2(x[k], pkbf(ga[k] * blo(z), gb[k] * bhi(z)));
        ga[k] = blo(z); gb[k] = bhi(z);
        const float ea = blo(x[k]) - s.ma, eb = bhi(x[k]) - s.mb;
        const u32 pd = pkbf(ea, eb);
        const u32 u = pkbf(blo(pd) * s.ra, bhi(pd) * s.rb);
        const float dk = DSK(k);
        yd[k] = pkbf(blo(u) * dk, bhi(u) * dk);
        ca[k] = ea + blo(yd[k]);
        cb[k] = eb + bhi(yd[k]);
        pin(ca[k]); pin(cb[k]); pin(ga[k]); pin(gb[k]);
    }
    wait_vm0();
    lsig(c_m2);
    if (b == 0 && t == 256) st8(ws, WS_EPOCH, 0, tag);
    SETPRIO(3);
    STAMP(12);
    if (!lpeek(smem + C_Y, 4 * half + 12)) {
        lwait(smem + C_Y, 4 * half + 12);
        __builtin_amdgcn_s_sleep(DLY_Y);
    }
    STAMP(15);
    get_y(ws, o_yr, s_yr + MB2, s_yr + MB2 + (YL ? 4096u : 8192u), y, dead);
    lsig(c_m1);
    lwait(smem + C_CL, 8);
    STAMP(16);
#pragma unroll
    for (int k = 0; k < 8; ++k) { da[k] = blo(y[k]) + ca[k]; db[k] = bhi(y[k]) + cb[k]; }
    Stat s2 = s;
    row_stats(smem + L_RR + 512, r, lane, da, db, s2, na, nb);
#pragma unroll
    for (int k = 0; k < 8; ++k) {
        da[k] = __builtin_fmaf(__builtin_fmaf(da[k], s2.ra, na), ga[k], da[k]);
        db[k] = __builtin_fmaf(__builtin_fmaf(db[k], s2.rb, nb), gb[k], db[k]);
    }
    STAMP(17);
    row_stats(smem + L_RR + 768, r, lane, da, db, s, na, nb);
    put_u(ws, o_w, s_w + 2 * MB2, da, db, s, na, nb);
    STAMP(18);
    SETPRIO(0);
    {
        char* tr = smem + L_SCR + r * 4352;
#pragma unroll
        for (int k = 0; k < 8; ++k) {
            u32 xx = add2(x[k], add2(y[k], yd[k]));
            const u32 n = ln2(xx, s2);
            xx = add2(xx, pkbf(blo(n) * ga[k], bhi(n) * gb[k]));
            const u32 u = ln2(xx, s);
            const float dk = DSK(k);
            const u32 d = pkbf(blo(u) * dk, bhi(u) * dk);
            const int co = (64 * (k >> 2) + 32 * (k & 1) + ((k >> 1) & 1)) * 4;
            *(float*)(tr + lg * 1088 + li * 8 + co) = blo(xx) + blo(d);
            *(float*)(tr + lg * 1088 + 544 + li * 8 + co) = bhi(xx) + bhi(d);
        }
        wait_vm0();
        lsig(c_m2);
#pragma unroll
        for (int e = 0; e < 2; ++e) {
            const v4f c0 = *(const v4f*)(tr + lg * 1088 + li * 32 + e * 16), c1 = *(const v4f*)(tr + lg * 1088 + 544 + li * 32 + e * 16);
#pragma unroll
            for (int k = 0; k < 4; ++k) { ca[e * 4 + k] = c0[k]; cb[e * 4 + k] = c1[k]; pin(ca[e * 4 + k]); pin(cb[e * 4 + k]); }
        }
    }
    SETPRIO(3);
    STAMP(19);
    if (!lpeek(smem + C_Y, 4 * half + 20)) {
        lwait(smem + C_Y, 4 * half + 20);
        __builtin_amdgcn_s_sleep(DLY_Y);
    }
    STAMP(22);
    get_y(ws, (u32)(li & 8) * 1024u + (u32)(li & 7) * 512u + (u32)(lg >> 1) * 128u + (u32)(lg & 1) * 4096u, s_yr + 2 * MB2, s_yr + 2 * MB2 + 256u, y, dead);
    STAMP(23);
    {
        const rsrc_t ro = mkr(p.out, SL * DM * 2);
        const u32 oo = (u32)lg * 2048u + (u32)li * 16u;
        v4u o0, o1;
#pragma unroll
        for (int k = 0; k < 4; ++k) {
            o0[k] = pkbf(blo(y[2 * k]) + ca[2 * k], blo(y[2 * k + 1]) + ca[2 * k + 1]);
            o1[k] = pkbf(bhi(y[2 * k]) + cb[2 * k], bhi(y[2 * k + 1]) + cb[2 * k + 1]);
        }
        st16(ro, oo, s_rw, o0);
        st16(ro, oo + 1024u, s_rw, o1);
    }
    STAMP(28);
}
#if defined(__HIP_DEVICE_COMPILE__)
#pragma clang attribute pop
#endif

extern "C" void kernel_launch(void* const* d_in, const int* in_sizes, int n_in,
                              void* d_out, int out_size, void* d_ws, size_t ws_size,
                              hipStream_t stream) {
    Params p;
    p.q = (const u16*)d_in[0];
    p.k = (const u16*)d_in[1];
    p.v = (const u16*)d_in[2];
    p.sk = (const u16*)d_in[3];
    p.ssmk = (const u16*)d_in[4];
    p.ssmD = (const u16*)d_in[5];
    p.logA = (const u16*)d_in[7];
    p.Aim = (const u16*)d_in[8];
    p.Cr = (const u16*)d_in[9];
    p.Ci = (const u16*)d_in[10];
    p.logdt = (const u16*)d_in[11];
    p.Dskip = (const u16*)d_in[12];
    p.out = (u16*)d_out;
    p.ws = (char*)d_ws;
    mega<<<256, 512, 0, stream>>>(p);
}
```

```cpp
#if defined(__HIP_DEVICE_COMPILE__)
#pragma clang attribute push(__attribute__((target("no-packed-fp32-ops"))), apply_to = function)
#endif
#include <hip/hip_runtime.h>
#include <stdint.h>

typedef unsigned short u16;
typedef unsigned u32;
typedef unsigned long long u64;
typedef u32 v4u __attribute__((ext_vector_type(4)));
typedef u32 v2u __attribute__((ext_vector_type(2)));
typedef float v4f __attribute__((ext_vector_type(4)));
typedef float v2f __attribute__((ext_vector_type(2)));
typedef __bf16 v2bf __attribute__((ext_vector_type(2)));
typedef __bf16 v8bf __attribute__((ext_vector_type(8)));
typedef _Float16 v8h __attribute__((ext_vector_type(8)));
typedef _Float16 v2h __attribute__((ext_vector_type(2)));
#define GAS __attribute__((address_space(1)))
#define LAS __attribute__((address_space(3)))
typedef GAS u64 gu64;
#define RLX_AGENT __ATOMIC_RELAXED, __HIP_MEMORY_SCOPE_AGENT

constexpr int SL = 2048, DM = 512, NST = 128;
constexpr u32 SENT = 0xFFFFFFFFu;
constexpr u32 SPIN_CAP = 1u << 15;

constexpr u32 WS_EPOCH = 0;
constexpr u32 WS_FLAG = 4096;
constexpr u32 MB1 = 1u << 20;
constexpr u32 MB2 = (2u << 20) + 65536u;
constexpr u32 WS_U = 65536u;
constexpr u32 WS_Y = WS_U + 3u * MB2;
constexpr u32 WS_BYTES = WS_U + 6u * MB2;
constexpr u32 LSPIN_CAP = 1u << 20;

constexpr int L_RR = 0;
constexpr int L_CNT = 1024;
constexpr int C_CONV = L_CNT, C_ROW = L_CNT + 64, C_Y = L_CNT + 128, C_K = L_CNT + 192, C_K1 = L_CNT + 224, L_YSC = L_CNT + 240, C_RY = L_CNT + 96, C_DUM = L_CNT + 160, C_CL = L_CNT + 32;
constexpr u32 LINC = 1u << 17;
constexpr int T_CH = 8 * 258 * 16;
constexpr int L_T = 1280;
constexpr int SCR_ROW = 528;
constexpr int SCR_CH = 57 * SCR_ROW;
constexpr int L_SCR = L_T + 2 * T_CH;
constexpr int U_CH = 320 * 16;
constexpr int L_U = L_SCR + 2 * SCR_CH;
constexpr int L_KTAB = L_U + 4 * U_CH;
constexpr int L_KL = L_KTAB + 2 * 1536;
constexpr int KL_CH = 257 * 16;
constexpr int L_TOTAL0 = L_KL + 2 * KL_CH;

constexpr int L_TOTAL = L_TOTAL0;
#define STAMP(i) do {} while (0)
#define STAMP_OUT() do {} while (0)
static_assert(L_TOTAL <= 163840, "lds");
struct Params {
    const u16 *q, *k, *v, *sk, *ssmk, *ssmD, *logA, *Aim, *Cr, *Ci, *logdt, *Dskip;
    u16* out;
    char* ws;
};

#define TLOAD_ASM \
    "ds_read_b128 a[0:3], %0 offset:4032\n\t" \
    "ds_read_b128 a[32:35], %0 offset:3520\n\t" \
    "ds_read_b128 a[64:67], %0 offset:3008\n\t" \
    "ds_read_b128 a[96:99], %0 offset:2496\n\t" \
    "ds_read_b128 a[4:7], %0 offset:3968\n\t" \
    "ds_read_b128 a[36:39], %0 offset:3456\n\t" \
    "ds_read_b128 a[68:71], %0 offset:2944\n\t" \
    "ds_read_b128 a[100:103], %0 offset:2432\n\t" \
    "ds_read_b128 a[8:11], %0 offset:3904\n\t" \
    "ds_read_b128 a[40:43], %0 offset:3392\n\t" \
    "ds_read_b128 a[72:75], %0 offset:2880\n\t" \
    "ds_read_b128 a[104:107], %0 offset:2368\n\t" \
    "ds_read_b128 a[12:15], %0 offset:3840\n\t" \
    "ds_read_b128 a[44:47], %0 offset:3328\n\t" \
    "ds_read_b128 a[76:79], %0 offset:2816\n\t" \
    "ds_read_b128 a[108:111], %0 offset:2304\n\t" \
    "ds_read_b128 a[16:19], %0 offset:3776\n\t" \
    "ds_read_b128 a[48:51], %0 offset:3264\n\t" \
    "ds_read_b128 a[80:83], %0 offset:2752\n\t" \
    "ds_read_b128 a[112:115], %0 offset:2240\n\t" \
    "ds_read_b128 a[20:23], %0 offset:3712\n\t" \
    "ds_read_b128 a[52:55], %0 offset:3200\n\t" \
    "ds_read_b128 a[84:87], %0 offset:2688\n\t" \
    "ds_read_b128 a[116:119], %0 offset:2176\n\t" \
    "ds_read_b128 a[24:27], %0 offset:3648\n\t" \
    "ds_read_b128 a[56:59], %0 offset:3136\n\t" \
    "ds_read_b128 a[88:91], %0 offset:2624\n\t" \
    "ds_read_b128 a[120:123], %0 offset:2112\n\t" \
    "ds_read_b128 a[28:31], %0 offset:3584\n\t" \
    "ds_read_b128 a[60:63], %0 offset:3072\n\t" \
    "ds_read_b128 a[92:95], %0 offset:2560\n\t" \
    "ds_read_b128 a[124:127], %0 offset:2048\n\t"
#define TLOAD_CLOB "a0", "a1", "a2", "a3", "a4", "a5", "a6", "a7", "a8", "a9", "a10", "a11", "a12", "a13", "a14", "a15", "a16", "a17", "a18", "a19", "a20", "a21", "a22", "a23", "a24", "a25", "a26", "a27", "a28", "a29", "a30", "a31", "a32", "a33", "a34", "a35", "a36", "a37", "a38", "a39", "a40", "a41", "a42", "a43", "a44", "a45", "a46", "a47", "a48", "a49", "a50", "a51", "a52", "a53", "a54", "a55", "a56", "a57", "a58", "a59", "a60", "a61", "a62", "a63", "a64", "a65", "a66", "a67", "a68", "a69", "a70", "a71", "a72", "a73", "a74", "a75", "a76", "a77", "a78", "a79", "a80", "a81", "a82", "a83", "a84", "a85", "a86", "a87", "a88", "a89", "a90", "a91", "a92", "a93", "a94", "a95", "a96", "a97", "a98", "a99", "a100", "a101", "a102", "a103", "a104", "a105", "a106", "a107", "a108", "a109", "a110", "a111", "a112", "a113", "a114", "a115", "a116", "a117", "a118", "a119", "a120", "a121", "a122", "a123", "a124", "a125", "a126", "a127"
#define CONV_CLOB "v64", "v65", "v66", "v67", "v68", "v69", "v70", "v71", "v72", "v73", "v74", "v75", "v76", "v77", "v78", "v79", "v80", "v81", "v82", "v83", "v84", "v85", "v86", "v87", "v88", "v89", "v90", "v91", "v92", "v93", "v94", "v95", "v96", "v97", "v98", "v99", "v100", "v101", "v102", "v103", "v104", "v105", "v106", "v107", "v108", "v109", "v110", "v111", "v112", "v113", "v114", "v115", "v116", "v117", "v118", "v119", "v120", "v121", "v122", "v123", "v124", "v125", "v126", "v127"
#define TRI_ASM \
    "1:\n\t" \
    "s_sub_u32 %4, %4, 1\n\t" \
    "ds_read_b32 v112, %6\n\t" \
    "ds_read_b128 v[64:67], %7 offset:1984\n\t" \
    "ds_read_b128 v[68:71], %7 offset:1472\n\t" \
    "ds_read_b128 v[72:75], %7 offset:960\n\t" \
    "ds_read_b128 v[76:79], %7 offset:448\n\t" \
    "ds_read_b128 v[80:83], %7 offset:1920\n\t" \
    "ds_read_b128 v[84:87], %7 offset:1408\n\t" \
    "ds_read_b128 v[88:91], %7 offset:896\n\t" \
    "ds_read_b128 v[92:95], %7 offset:384\n\t" \
    "ds_read_b128 v[96:99], %7 offset:1856\n\t" \
    "ds_read_b128 v[100:103], %7 offset:1344\n\t" \
    "ds_read_b128 v[104:107], %7 offset:832\n\t" \
    "ds_read_b128 v[108:111], %7 offset:320\n\t" \
    "s_waitcnt lgkmcnt(12)\n\t" \
    "v_readfirstlane_b32 %5, v112\n\t" \
    "s_cmp_lt_u32 %5, %4\n\t" \
    "s_cbranch_scc1 1b\n\t" \
    "s_waitcnt lgkmcnt(11)\n\t" \
    "v_mfma_f32_16x16x32_bf16 %2, a[0:3], v[64:67], %2\n\t" \
    "ds_read_b128 v[112:115], %7 offset:1792\n\t" \
    "s_waitcnt lgkmcnt(11)\n\t" \
    "v_mfma_f32_16x16x32_bf16 %3, a[32:35], v[68:71], %3\n\t" \
    "ds_read_b128 v[116:119], %7 offset:1280\n\t" \
    "s_waitcnt lgkmcnt(11)\n\t" \
    "v_mfma_f32_16x16x32_bf16 %0, a[0:3], v[72:75], %0\n\t" \
    "ds_read_b128 v[120:123], %7 offset:768\n\t" \
    "v_mfma_f32_16x16x32_bf16 %2, a[64:67], v[72:75], %2\n\t" \
    "ds_read_b128 v[124:127], %7 offset:256\n\t" \
    "s_waitcnt lgkmcnt(12)\n\t" \
    "v_mfma_f32_16x16x32_bf16 %1, a[32:35], v[76:79], %1\n\t" \
    "v_mfma_f32_16x16x32_bf16 %3, a[96:99], v[76:79], %3\n\t" \
    "s_waitcnt lgkmcnt(10)\n\t" \
    "v_mfma_f32_16x16x32_bf16 %2, a[4:7], v[80:83], %2\n\t" \
    "ds_read_b128 v[64:67], %7 offset:1728\n\t" \
    "v_mfma_f32_16x16x32_bf16 %3, a[36:39], v[84:87], %3\n\t" \
    "ds_read_b128 v[68:71], %7 offset:1216\n\t" \
    "s_waitcnt lgkmcnt(10)\n\t" \
    "v_mfma_f32_16x16x32_bf16 %0, a[4:7], v[88:91], %0\n\t" \
    "ds_read_b128 v[72:75], %7 offset:704\n\t" \
    "v_mfma_f32_16x16x32_bf16 %2, a[68:71], v[88:91], %2\n\t" \
    "ds_read_b128 v[76:79], %7 offset:192\n\t" \
    "v_mfma_f32_16x16x32_bf16 %1, a[36:39], v[92:95], %1\n\t" \
    "v_mfma_f32_16x16x32_bf16 %3, a[100:103], v[92:95], %3\n\t" \
    "s_waitcnt lgkmcnt(10)\n\t" \
    "v_mfma_f32_16x16x32_bf16 %2, a[8:11], v[96:99], %2\n\t" \
    "ds_read_b128 v[80:83], %7 offset:1664\n\t" \
    "v_mfma_f32_16x16x32_bf16 %3, a[40:43], v[100:103], %3\n\t" \
    "ds_read_b128 v[84:87], %7 offset:1152\n\t" \
    "s_waitcnt lgkmcnt(10)\n\t" \
    "v_mfma_f32_16x16x32_bf16 %0, a[8:11], v[104:107], %0\n\t" \
    "ds_read_b128 v[88:91], %7 offset:640\n\t" \
    "v_mfma_f32_16x16x32_bf16 %2, a[72:75], v[104:107], %2\n\t" \
    "ds_read_b128 v[92:95], %7 offset:128\n\t" \
    "v_mfma_f32_16x16x32_bf16 %1, a[40:43], v[108:111], %1\n\t" \
    "v_mfma_f32_16x16x32_bf16 %3, a[104:107], v[108:111], %3\n\t" \
    "s_waitcnt lgkmcnt(10)\n\t" \
    "v_mfma_f32_16x16x32_bf16 %2, a[12:15], v[112:115], %2\n\t" \
    "ds_read_b128 v[96:99], %7 offset:1600\n\t" \
    "v_mfma_f32_16x16x32_bf16 %3, a[44:47], v[116:119], %3\n\t" \
    "ds_read_b128 v[100:103], %7 offset:1088\n\t" \
    "s_waitcnt lgkmcnt(10)\n\t" \
    "v_mfma_f32_16x16x32_bf16 %0, a[12:15], v[120:123], %0\n\t" \
    "ds_read_b128 v[104:107], %7 offset:576\n\t" \
    "v_mfma_f32_16x16x32_bf16 %2, a[76:79], v[120:123], %2\n\t" \
    "ds_read_b128 v[108:111], %7 offset:64\n\t" \
    "v_mfma_f32_16x16x32_bf16 %1, a[44:47], v[124:127], %1\n\t" \
    "v_mfma_f32_16x16x32_bf16 %3, a[108:111], v[124:127], %3\n\t" \
    "s_waitcnt lgkmcnt(10)\n\t" \
    "v_mfma_f32_16x16x32_bf16 %2, a[16:19], v[64:67], %2\n\t" \
    "ds_read_b128 v[112:115], %7 offset:1536\n\t" \
    "v_mfma_f32_16x16x32_bf16 %3, a[48:51], v[68:71], %3\n\t" \
    "ds_read_b128 v[116:119], %7 offset:1024\n\t" \
    "s_waitcnt lgkmcnt(10)\n\t" \
    "v_mfma_f32_16x16x32_bf16 %0, a[16:19], v[72:75], %0\n\t" \
    "ds_read_b128 v[120:123], %7 offset:512\n\t" \
    "v_mfma_f32_16x16x32_bf16 %2, a[80:83], v[72:75], %2\n\t" \
    "ds_read_b128 v[124:127], %7 offset:0\n\t" \
    "v_mfma_f32_16x16x32_bf16 %1, a[48:51], v[76:79], %1\n\t" \
    "v_mfma_f32_16x16x32_bf16 %3, a[112:115], v[76:79], %3\n\t" \
    "s_waitcnt lgkmcnt(10)\n\t" \
    "v_mfma_f32_16x16x32_bf16 %2, a[20:23], v[80:83], %2\n\t" \
    "v_mfma_f32_16x16x32_bf16 %3, a[52:55], v[84:87], %3\n\t" \
    "s_waitcnt lgkmcnt(8)\n\t" \
    "v_mfma_f32_16x16x32_bf16 %0, a[20:23], v[88:91], %0\n\t" \
    "v_mfma_f32_16x16x32_bf16 %2, a[84:87], v[88:91], %2\n\t" \
    "v_mfma_f32_16x16x32_bf16 %1, a[52:55], v[92:95], %1\n\t" \
    "v_mfma_f32_16x16x32_bf16 %3, a[116:119], v[92:95], %3\n\t" \
    "s_waitcnt lgkmcnt(6)\n\t" \
    "v_mfma_f32_16x16x32_bf16 %2, a[24:27], v[96:99], %2\n\t" \
    "v_mfma_f32_16x16x32_bf16 %3, a[56:59], v[100:103], %3\n\t" \
    "s_waitcnt lgkmcnt(4)\n\t" \
    "v_mfma_f32_16x16x32_bf16 %0, a[24:27], v[104:107], %0\n\t" \
    "v_mfma_f32_16x16x32_bf16 %2, a[88:91], v[104:107], %2\n\t" \
    "v_mfma_f32_16x16x32_bf16 %1, a[56:59], v[108:111], %1\n\t" \
    "v_mfma_f32_16x16x32_bf16 %3, a[120:123], v[108:111], %3\n\t" \
    "s_waitcnt lgkmcnt(2)\n\t" \
    "v_mfma_f32_16x16x32_bf16 %2, a[28:31], v[112:115], %2\n\t" \
    "v_mfma_f32_16x16x32_bf16 %3, a[60:63], v[116:119], %3\n\t" \
    "s_waitcnt lgkmcnt(0)\n\t" \
    "v_mfma_f32_16x16x32_bf16 %0, a[28:31], v[120:123], %0\n\t" \
    "v_mfma_f32_16x16x32_bf16 %2, a[92:95], v[120:123], %2\n\t" \
    "v_mfma_f32_16x16x32_bf16 %1, a[60:63], v[124:127], %1\n\t" \
    "v_mfma_f32_16x16x32_bf16 %3, a[124:127], v[124:127], %3\n\t" \
    "s_nop 7\n\t" \
    "s_nop 1\n\t"
#define CROSS_ASM \
    "ds_read_b128 v[64:67], %5 offset:1984\n\t" \
    "ds_read_b128 v[68:71], %5 offset:1472\n\t" \
    "ds_read_b128 v[72:75], %6 offset:1984\n\t" \
    "ds_read_b128 v[76:79], %6 offset:1472\n\t" \
    "ds_read_b128 v[80:83], %5 offset:960\n\t" \
    "ds_read_b128 v[84:87], %5 offset:448\n\t" \
    "ds_read_b128 v[88:91], %6 offset:960\n\t" \
    "ds_read_b128 v[92:95], %6 offset:448\n\t" \
    "ds_read_b128 v[96:99], %5 offset:1920\n\t" \
    "ds_read_b128 v[100:103], %5 offset:1408\n\t" \
    "ds_read_b128 v[104:107], %6 offset:1920\n\t" \
    "ds_read_b128 v[108:111], %6 offset:1408\n\t" \
    "ds_read_b128 v[112:115], %5 offset:896\n\t" \
    "s_waitcnt lgkmcnt(11)\n\t" \
    "v_mfma_f32_16x16x32_bf16 %0, a[64:67], v[64:67], %0\n\t" \
    "ds_read_b128 v[116:119], %5 offset:384\n\t" \
    "v_mfma_f32_16x16x32_bf16 %1, a[96:99], v[68:71], %1\n\t" \
    "ds_read_b128 v[120:123], %6 offset:896\n\t" \
    "s_waitcnt lgkmcnt(11)\n\t" \
    "v_mfma_f32_16x16x32_bf16 %2, v[72:75], v[64:67], %2\n\t" \
    "ds_read_b128 v[124:127], %6 offset:384\n\t" \
    "v_mfma_f32_16x16x32_bf16 %3, v[76:79], v[68:71], %3\n\t" \
    "ds_read_b128 v[64:67], %5 offset:1856\n\t" \
    "s_waitcnt lgkmcnt(11)\n\t" \
    "v_mfma_f32_16x16x32_bf16 %0, v[72:75], v[80:83], %0\n\t" \
    "ds_read_b128 v[68:71], %5 offset:1344\n\t" \
    "v_mfma_f32_16x16x32_bf16 %1, v[76:79], v[84:87], %1\n\t" \
    "ds_read_b128 v[72:75], %6 offset:1856\n\t" \
    "s_waitcnt lgkmcnt(11)\n\t" \
    "v_mfma_f32_16x16x32_bf16 %2, v[88:91], v[80:83], %2\n\t" \
    "ds_read_b128 v[76:79], %6 offset:1344\n\t" \
    "v_mfma_f32_16x16x32_bf16 %3, v[92:95], v[84:87], %3\n\t" \
    "ds_read_b128 v[80:83], %5 offset:832\n\t" \
    "s_waitcnt lgkmcnt(11)\n\t" \
    "v_mfma_f32_16x16x32_bf16 %0, a[68:71], v[96:99], %0\n\t" \
    "ds_read_b128 v[84:87], %5 offset:320\n\t" \
    "v_mfma_f32_16x16x32_bf16 %1, a[100:103], v[100:103], %1\n\t" \
    "ds_read_b128 v[88:91], %6 offset:832\n\t" \
    "s_waitcnt lgkmcnt(11)\n\t" \
    "v_mfma_f32_16x16x32_bf16 %2, v[104:107], v[96:99], %2\n\t" \
    "ds_read_b128 v[92:95], %6 offset:320\n\t" \
    "v_mfma_f32_16x16x32_bf16 %3, v[108:111], v[100:103], %3\n\t" \
    "ds_read_b128 v[96:99], %5 offset:1792\n\t" \
    "s_waitcnt lgkmcnt(11)\n\t" \
    "v_mfma_f32_16x16x32_bf16 %0, v[104:107], v[112:115], %0\n\t" \
    "ds_read_b128 v[100:103], %5 offset:1280\n\t" \
    "v_mfma_f32_16x16x32_bf16 %1, v[108:111], v[116:119], %1\n\t" \
    "ds_read_b128 v[104:107], %6 offset:1792\n\t" \
    "s_waitcnt lgkmcnt(11)\n\t" \
    "v_mfma_f32_16x16x32_bf16 %2, v[120:123], v[112:115], %2\n\t" \
    "ds_read_b128 v[108:111], %6 offset:1280\n\t" \
    "v_mfma_f32_16x16x32_bf16 %3, v[124:127], v[116:119], %3\n\t" \
    "ds_read_b128 v[112:115], %5 offset:768\n\t" \
    "s_waitcnt lgkmcnt(11)\n\t" \
    "v_mfma_f32_16x16x32_bf16 %0, a[72:75], v[64:67], %0\n\t" \
    "ds_read_b128 v[116:119], %5 offset:256\n\t" \
    "v_mfma_f32_16x16x32_bf16 %1, a[104:107], v[68:71], %1\n\t" \
    "ds_read_b128 v[120:123], %6 offset:768\n\t" \
    "s_waitcnt lgkmcnt(11)\n\t" \
    "v_mfma_f32_16x16x32_bf16 %2, v[72:75], v[64:67], %2\n\t" \
    "ds_read_b128 v[124:127], %6 offset:256\n\t" \
    "v_mfma_f32_16x16x32_bf16 %3, v[76:79], v[68:71], %3\n\t" \
    "ds_read_b128 v[64:67], %5 offset:1728\n\t" \
    "s_waitcnt lgkmcnt(11)\n\t" \
    "v_mfma_f32_16x16x32_bf16 %0, v[72:75], v[80:83], %0\n\t" \
    "buffer_load_dwordx4 %4, %7, %8, %9 offen sc1\n\t" \
    "ds_read_b128 v[68:71], %5 offset:1216\n\t" \
    "v_mfma_f32_16x16x32_bf16 %1, v[76:79], v[84:87], %1\n\t" \
    "ds_read_b128 v[72:75], %6 offset:1728\n\t" \
    "s_waitcnt lgkmcnt(11)\n\t" \
    "v_mfma_f32_16x16x32_bf16 %2, v[88:91], v[80:83], %2\n\t" \
    "ds_read_b128 v[76:79], %6 offset:1216\n\t" \
    "v_mfma_f32_16x16x32_bf16 %3, v[92:95], v[84:87], %3\n\t" \
    "ds_read_b128 v[80:83], %5 offset:704\n\t" \
    "s_waitcnt lgkmcnt(11)\n\t" \
    "v_mfma_f32_16x16x32_bf16 %0, a[76:79], v[96:99], %0\n\t" \
    "ds_read_b128 v[84:87], %5 offset:192\n\t" \
    "v_mfma_f32_16x16x32_bf16 %1, a[108:111], v[100:103], %1\n\t" \
    "ds_read_b128 v[88:91], %6 offset:704\n\t" \
    "s_waitcnt lgkmcnt(11)\n\t" \
    "v_mfma_f32_16x16x32_bf16 %2, v[104:107], v[96:99], %2\n\t" \
    "ds_read_b128 v[92:95], %6 offset:192\n\t" \
    "v_mfma_f32_16x16x32_bf16 %3, v[108:111], v[100:103], %3\n\t" \
    "ds_read_b128 v[96:99], %5 offset:1664\n\t" \
    "s_waitcnt lgkmcnt(11)\n\t" \
    "v_mfma_f32_16x16x32_bf16 %0, v[104:107], v[112:115], %0\n\t" \
    "ds_read_b128 v[100:103], %5 offset:1152\n\t" \
    "v_mfma_f32_16x16x32_bf16 %1, v[108:111], v[116:119], %1\n\t" \
    "ds_read_b128 v[104:107], %6 offset:1664\n\t" \
    "s_waitcnt lgkmcnt(11)\n\t" \
    "v_mfma_f32_16x16x32_bf16 %2, v[120:123], v[112:115], %2\n\t" \
    "ds_read_b128 v[108:111], %6 offset:1152\n\t" \
    "v_mfma_f32_16x16x32_bf16 %3, v[124:127], v[116:119], %3\n\t" \
    "ds_read_b128 v[112:115], %5 offset:640\n\t" \
    "s_waitcnt lgkmcnt(11)\n\t" \
    "v_mfma_f32_16x16x32_bf16 %0, a[80:83], v[64:67], %0\n\t" \
    "ds_read_b128 v[116:119], %5 offset:128\n\t" \
    "v_mfma_f32_16x16x32_bf16 %1, a[112:115], v[68:71], %1\n\t" \
    "ds_read_b128 v[120:123], %6 offset:640\n\t" \
    "s_waitcnt lgkmcnt(11)\n\t" \
    "v_mfma_f32_16x16x32_bf16 %2, v[72:75], v[64:67], %2\n\t" \
    "ds_read_b128 v[124:127], %6 offset:128\n\t" \
    "v_mfma_f32_16x16x32_bf16 %3, v[76:79], v[68:71], %3\n\t" \
    "ds_read_b128 v[64:67], %5 offset:1600\n\t" \
    "s_waitcnt lgkmcnt(11)\n\t" \
    "v_mfma_f32_16x16x32_bf16 %0, v[72:75], v[80:83], %0\n\t" \
    "ds_read_b128 v[68:71], %5 offset:1088\n\t" \
    "v_mfma_f32_16x16x32_bf16 %1, v[76:79], v[84:87], %1\n\t" \
    "ds_read_b128 v[72:75], %6 offset:1600\n\t" \
    "s_waitcnt lgkmcnt(11)\n\t" \
    "v_mfma_f32_16x16x32_bf16 %2, v[88:91], v[80:83], %2\n\t" \
    "ds_read_b128 v[76:79], %6 offset:1088\n\t" \
    "v_mfma_f32_16x16x32_bf16 %3, v[92:95], v[84:87], %3\n\t" \
    "ds_read_b128 v[80:83], %5 offset:576\n\t" \
    "s_waitcnt lgkmcnt(11)\n\t" \
    "v_mfma_f32_16x16x32_bf16 %0, a[84:87], v[96:99], %0\n\t" \
    "ds_read_b128 v[84:87], %5 offset:64\n\t" \
    "v_mfma_f32_16x16x32_bf16 %1, a[116:119], v[100:103], %1\n\t" \
    "ds_read_b128 v[88:91], %6 offset:576\n\t" \
    "s_waitcnt lgkmcnt(11)\n\t" \
    "v_mfma_f32_16x16x32_bf16 %2, v[104:107], v[96:99], %2\n\t" \
    "ds_read_b128 v[92:95], %6 offset:64\n\t" \
    "v_mfma_f32_16x16x32_bf16 %3, v[108:111], v[100:103], %3\n\t" \
    "ds_read_b128 v[96:99], %5 offset:1536\n\t" \
    "s_waitcnt lgkmcnt(11)\n\t" \
    "v_mfma_f32_16x16x32_bf16 %0, v[104:107], v[112:115], %0\n\t" \
    "ds_read_b128 v[100:103], %5 offset:1024\n\t" \
    "v_mfma_f32_16x16x32_bf16 %1, v[108:111], v[116:119], %1\n\t" \
    "ds_read_b128 v[104:107], %6 offset:1536\n\t" \
    "s_waitcnt lgkmcnt(11)\n\t" \
    "v_mfma_f32_16x16x32_bf16 %2, v[120:123], v[112:115], %2\n\t" \
    "ds_read_b128 v[108:111], %6 offset:1024\n\t" \
    "v_mfma_f32_16x16x32_bf16 %3, v[124:127], v[116:119], %3\n\t" \
    "ds_read_b128 v[112:115], %5 offset:512\n\t" \
    "s_waitcnt lgkmcnt(11)\n\t" \
    "v_mfma_f32_16x16x32_bf16 %0, a[88:91], v[64:67], %0\n\t" \
    "ds_read_b128 v[116:119], %5 offset:0\n\t" \
    "v_mfma_f32_16x16x32_bf16 %1, a[120:123], v[68:71], %1\n\t" \
    "ds_read_b128 v[120:123], %6 offset:512\n\t" \
    "s_waitcnt lgkmcnt(11)\n\t" \
    "v_mfma_f32_16x16x32_bf16 %2, v[72:75], v[64:67], %2\n\t" \
    "ds_read_b128 v[124:127], %6 offset:0\n\t" \
    "v_mfma_f32_16x16x32_bf16 %3, v[76:79], v[68:71], %3\n\t" \
    "s_waitcnt lgkmcnt(10)\n\t" \
    "v_mfma_f32_16x16x32_bf16 %0, v[72:75], v[80:83], %0\n\t" \
    "v_mfma_f32_16x16x32_bf16 %1, v[76:79], v[84:87], %1\n\t" \
    "s_waitcnt lgkmcnt(8)\n\t" \
    "v_mfma_f32_16x16x32_bf16 %2, v[88:91], v[80:83], %2\n\t" \
    "v_mfma_f32_16x16x32_bf16 %3, v[92:95], v[84:87], %3\n\t" \
    "s_waitcnt lgkmcnt(6)\n\t" \
    "v_mfma_f32_16x16x32_bf16 %0, a[92:95], v[96:99], %0\n\t" \
    "v_mfma_f32_16x16x32_bf16 %1, a[124:127], v[100:103], %1\n\t" \
    "s_waitcnt lgkmcnt(4)\n\t" \
    "v_mfma_f32_16x16x32_bf16 %2, v[104:107], v[96:99], %2\n\t" \
    "v_mfma_f32_16x16x32_bf16 %3, v[108:111], v[100:103], %3\n\t" \
    "s_waitcnt lgkmcnt(2)\n\t" \
    "v_mfma_f32_16x16x32_bf16 %0, v[104:107], v[112:115], %0\n\t" \
    "v_mfma_f32_16x16x32_bf16 %1, v[108:111], v[116:119], %1\n\t" \
    "s_waitcnt lgkmcnt(0)\n\t" \
    "v_mfma_f32_16x16x32_bf16 %2, v[120:123], v[112:115], %2\n\t" \
    "v_mfma_f32_16x16x32_bf16 %3, v[124:127], v[116:119], %3\n\t" \
    "s_nop 7\n\t" \
    "s_nop 1\n\t" \
    "s_waitcnt vmcnt(0)\n\t"

__device__ __forceinline__ float u2f(u32 x) { return __builtin_bit_cast(float, x); }
__device__ __forceinline__ u32 f2u(float x) { return __builtin_bit_cast(u32, x); }
__device__ __forceinline__ float blo(u32 x) { return u2f(x << 16); }
__device__ __forceinline__ float bhi(u32 x) { return u2f(x & 0xffff0000u); }
__device__ __forceinline__ u32 pkbf(float a, float b) {
    v2f f = {a, b};
    return __builtin_bit_cast(u32, __builtin_convertvector(f, v2bf));
}
__device__ __forceinline__ float rbf(float a) { return blo(pkbf(a, a)); }
__device__ __forceinline__ u32 pkh(float a, float b) {
    v2f f = {a, b};
    return __builtin_bit_cast(u32, __builtin_convertvector(f, v2h));
}
template <int CTRL> __device__ __forceinline__ float dppf(float x) {
    return __builtin_bit_cast(float, __builtin_amdgcn_mov_dpp(__builtin_bit_cast(int, x), CTRL, 0xf, 0xf, true));
}
template <int CTRL> __device__ __forceinline__ u32 dppu(u32 x) {
    return (u32)__builtin_amdgcn_mov_dpp((int)x, CTRL, 0xf, 0xf, true);
}
constexpr int XOR1 = 0xB1, XOR2 = 0x4E, HMIR = 0x141, MIR = 0x140;
__device__ __forceinline__ void bar() { asm volatile("s_waitcnt lgkmcnt(0)\n\ts_barrier" ::: "memory"); }
__device__ __forceinline__ void wait_vm0() { asm volatile("s_waitcnt vmcnt(0)" ::: "memory"); }
__device__ __forceinline__ u32 umin(u32 a, u32 b) { return a < b ? a : b; }
__device__ __forceinline__ u32 umax(u32 a, u32 b) { return a > b ? a : b; }
__device__ __forceinline__ u32 ns(u32 x) { return umin(x, SENT - 1u); }
__device__ __forceinline__ bool isvalid(v4u x) { return umax(umax(x.x, x.y), umax(x.z, x.w)) != SENT; }
template <class T> __device__ __forceinline__ void pin(T& x) { asm volatile("" : "+v"(x)); }

typedef __amdgpu_buffer_rsrc_t rsrc_t;
__device__ __forceinline__ rsrc_t mkr(const void* p, u32 bytes) { return __builtin_amdgcn_make_buffer_rsrc((void*)p, 0, bytes, 0x00020000); }
__device__ __forceinline__ void st16(rsrc_t r, u32 off, u32 so, v4u v) {
    asm volatile("" : "+v"(v));
    __builtin_amdgcn_raw_buffer_store_b128(v, r, off, so, 16);
    asm volatile("s_nop 1" :: "v"(v));
}
__device__ __forceinline__ void st8(rsrc_t r, u32 off, u32 so, v2u v) { __builtin_amdgcn_raw_buffer_store_b64(v, r, off, so, 16); }
__device__ __forceinline__ v4u ld16(rsrc_t r, u32 off, u32 so) { return __builtin_amdgcn_raw_buffer_load_b128(r, off, so, 16); }
__device__ __forceinline__ u32 ldw(rsrc_t r, u32 off, u32 so) { return __builtin_amdgcn_raw_buffer_load_b32(r, off, so, 0); }
__device__ __forceinline__ u32 ldh(rsrc_t r, u32 off, u32 so) { return (u32)__builtin_amdgcn_raw_buffer_load_b16(r, off, so, 0); }

__device__ __forceinline__ void wait_flag(rsrc_t r, u32 off, v2u tag, bool& dead) {
    u32 spins = 0;
    while (!dead) {
        asm volatile("" ::: "memory");
        const v2u x = __builtin_amdgcn_raw_buffer_load_b64(r, off, 0, 16);
        if (x.x == tag.x && x.y == tag.y) break;
        if (++spins > SPIN_CAP) dead = true;
    }
}
__device__ __forceinline__ void repoll16(rsrc_t r, u32 off, u32 so, v4u& x, bool& dead) {
    u32 spins = 0;
    while (!isvalid(x) && !dead) {
        asm volatile("" ::: "memory");
        x = ld16(r, off, so);
        if (++spins > SPIN_CAP) dead = true;
    }
}

__device__ __forceinline__ void build_T(char* base, v4u lo, v4u hi) {
    const u32 W[8] = {lo.x, lo.y, lo.z, lo.w, hi.x, hi.y, hi.z, hi.w};
    u32 R[8], B[8];
#pragma unroll
    for (int m = 1; m < 8; ++m) {
        R[m] = __builtin_amdgcn_alignbit(W[m], W[m], 16);
        B[m] = (W[m] & 0xffffu) | (W[m - 1] & 0xffff0000u);
    }
#pragma unroll
    for (int rho = 0; rho < 8; ++rho) {
        const int idx = rho < 4 ? 2 * rho : 2 * (rho - 4) + 1;
        v4u o;
        if (rho & 1) { const int m = (7 + rho) / 2; o = (v4u){R[m], R[m - 1], R[m - 2], R[m - 3]}; }
        else { const int m = 4 + rho / 2; o = (v4u){B[m], B[m - 1], B[m - 2], B[m - 3]}; }
        *(v4u*)(base + idx * 258 * 16) = o;
    }
}

__device__ __forceinline__ void to_rowlay(v4u a, u32 (&o)[4]) {
    auto s0 = __builtin_amdgcn_permlane32_swap(a.x, a.z, false, false);
    auto s1 = __builtin_amdgcn_permlane32_swap(a.y, a.w, false, false);
    auto s2 = __builtin_amdgcn_permlane16_swap(s0[0], s1[0], false, false);
    auto s3 = __builtin_amdgcn_permlane16_swap(s0[1], s1[1], false, false);
    o[0] = s2[0]; o[1] = s2[1]; o[2] = s3[0]; o[3] = s3[1];
}
__device__ __forceinline__ v4u from_rowlay(const u32 (&o)[4]) {
    auto s2 = __builtin_amdgcn_permlane16_swap(o[0], o[1], false, false);
    auto s3 = __builtin_amdgcn_permlane16_swap(o[2], o[3], false, false);
    auto s0 = __builtin_amdgcn_permlane32_swap(s2[0], s3[0], false, false);
    auto s1 = __builtin_amdgcn_permlane32_swap(s2[1], s3[1], false, false);
    return (v4u){s0[0], s1[0], s0[1], s1[1]};
}

__device__ __forceinline__ void lsig(u32 addr) {
    asm volatile("s_mov_b64 exec, 1\n\tds_add_u32 %0, %1\n\ts_mov_b64 exec, -1" :: "v"(addr), "v"(LINC) : "memory");
}
#define SETPRIO(x) __builtin_amdgcn_s_setprio(x)
__device__ __forceinline__ void lwait(const char* p, u32 target) {
    u32 tg = target * LINC + 1u;
    do {
        asm volatile("" ::: "memory");
        --tg;
    } while ((u32)__builtin_amdgcn_readfirstlane(*(const u32*)p) < tg);
    asm volatile("" ::: "memory");
}
__device__ __forceinline__ bool lpeek(const char* p, u32 target) {
    asm volatile("" ::: "memory");
    return (u32)__builtin_amdgcn_readfirstlane(*(const u32*)p) >= target * LINC;
}
#define POLL_HEAD "s_mov_b64 vcc, -1\n\t1:\n\ts_mov_b64 exec, vcc\n\t"
#define POLL_MID "s_mov_b64 exec, -1\n\ts_sub_i32 %0, %0, 1\n\ts_cmp_gt_i32 %0, 0\n\ts_cselect_b64 %1, -1, 0\n\t"
#define POLL_TAIL "s_and_b64 vcc, vcc, %1\n\ts_cbranch_scc1 1b\n\t"
__device__ __forceinline__ void poll2(rsrc_t ws, u32 off, u32 so0, u32 so1, v4u& a, v4u& b, int& bud) {
    u64 m; u32 t;
    asm volatile(POLL_HEAD
        "buffer_load_dwordx4 v[56:59], %5, %6, %7 offen sc1\n\t"
        "buffer_load_dwordx4 v[60:63], %5, %6, %8 offen sc1\n\t"
        POLL_MID
        "s_waitcnt vmcnt(1)\n\t"
        "v_max3_u32 %2, v56, v57, v58\n\t"
        "s_waitcnt vmcnt(0)\n\t"
        "v_max3_u32 %2, v59, v60, %2\n\t"
        "v_max3_u32 %2, v61, v62, %2\n\t"
        "v_max_u32 %2, v63, %2\n\t"
        "v_cmp_eq_u32 vcc, -1, %2\n\t"
        POLL_TAIL
        : "+s"(bud), "=&s"(m), "=&v"(t), "=&{v[56:59]}"(a), "=&{v[60:63]}"(b) : "v"(off), "s"(ws), "s"(so0), "s"(so1) : "memory", "vcc", "scc");
}
__device__ __forceinline__ void poll1(rsrc_t ws, u32 off, u32 so, v4u& a, int& bud) {
    u64 m; u32 t;
    asm volatile(POLL_HEAD
        "buffer_load_dwordx4 v[56:59], %4, %5, %6 offen sc1\n\t"
        POLL_MID
        "s_waitcnt vmcnt(0)\n\t"
        "v_max3_u32 %2, v56, v57, v58\n\t"
        "v_max_u32 %2, v59, %2\n\t"
        "v_cmp_eq_u32 vcc, -1, %2\n\t"
        POLL_TAIL
        : "+s"(bud), "=&s"(m), "=&v"(t), "=&{v[56:59]}"(a) : "v"(off), "s"(ws), "s"(so) : "memory", "vcc", "scc");
}
__device__ __forceinline__ void pollf(rsrc_t ws, u32 off, u32 so, u64 f, u64 tag, int& bud) {
    if (__builtin_amdgcn_ballot_w64(f != tag) != 0) {
        u64 m;
        asm volatile(POLL_HEAD
            "buffer_load_dwordx2 %2, %3, %4, %5 offen sc1\n\t"
            POLL_MID
            "s_waitcnt vmcnt(0)\n\t"
            "v_cmp_ne_u64 vcc, %6, %2\n\t"
            POLL_TAIL
            : "+s"(bud), "=&s"(m), "=&v"(f) : "v"(off), "s"(ws), "s"(so), "s"(tag) : "memory", "vcc", "scc");
    }
}
__device__ __forceinline__ u64 ldf(rsrc_t ws, u32 off, u32 so) { return __builtin_bit_cast(u64, __builtin_amdgcn_raw_buffer_load_b64(ws, off, so, 16)); }

struct Stat { float ma, mb, ra, rb; };
__device__ __forceinline__ void row_stats(char* rr, int r, int lane, const float (&da)[8], const float (&db)[8], Stat& s, float& na, float& nb) {
    float v0 = ((da[0] + da[1]) + (da[2] + da[3])) + ((da[4] + da[5]) + (da[6] + da[7]));
    float v2 = ((db[0] + db[1]) + (db[2] + db[3])) + ((db[4] + db[5]) + (db[6] + db[7]));
    float v1 = da[0] * da[0], v1b = da[1] * da[1], v3 = db[0] * db[0], v3b = db[1] * db[1];
#pragma unroll
    for (int k = 2; k < 8; k += 2) {
        v1 = __builtin_fmaf(da[k], da[k], v1); v1b = __builtin_fmaf(da[k + 1], da[k + 1], v1b);
        v3 = __builtin_fmaf(db[k], db[k], v3); v3b = __builtin_fmaf(db[k + 1], db[k + 1], v3b);
    }
    v1 += v1b; v3 += v3b;
    asm volatile(
        "s_nop 1\n\t"
        "v_add_f32_dpp %0, %0, %0 row_ror:8 row_mask:0xf bank_mask:0x3\n\t"
        "v_add_f32_dpp %1, %1, %1 row_ror:8 row_mask:0xf bank_mask:0x3\n\t"
        "v_add_f32_dpp %0, %2, %2 row_ror:8 row_mask:0xf bank_mask:0xc\n\t"
        "v_add_f32_dpp %1, %3, %3 row_ror:8 row_mask:0xf bank_mask:0xc\n\t"
        "s_nop 0\n\t"
        "v_add_f32_dpp %0, %0, %0 row_half_mirror row_mask:0xf bank_mask:0x5\n\t"
        "v_add_f32_dpp %0, %1, %1 row_half_mirror row_mask:0xf bank_mask:0xa\n\t"
        "s_nop 1\n\t"
        "v_add_f32_dpp %0, %0, %0 quad_perm:[1,0,3,2] row_mask:0xf bank_mask:0xf\n\t"
        "s_nop 1\n\t"
        "v_add_f32_dpp %0, %0, %0 quad_perm:[2,3,0,1] row_mask:0xf bank_mask:0xf\n\t"
        : "+v"(v0), "+v"(v1) : "v"(v2), "v"(v3));
    *(u32*)(rr + (lane >> 4) * 64 + r * 16 + (lane & 12)) = ns(f2u(v0));
    v4u zi;
    u32 spins = 0;
    do {
        asm volatile("" ::: "memory");
        zi = *(const v4u*)(rr + (lane >> 4) * 64 + (lane & 3) * 16);
    } while (__builtin_amdgcn_ballot_w64(!isvalid(zi)) != 0 && ++spins < LSPIN_CAP);
    v4f z = __builtin_bit_cast(v4f, zi);
#pragma unroll
    for (int k = 0; k < 4; ++k) z[k] += dppf<XOR1>(z[k]);
#pragma unroll
    for (int k = 0; k < 4; ++k) z[k] += dppf<XOR2>(z[k]);
    const float inv = 1.f / DM;
    const float mua = z[0] * inv, mub = z[2] * inv;
    const float va = __builtin_fmaxf(__builtin_fmaf(-mua, mua, z[1] * inv), 0.f), vb = __builtin_fmaxf(__builtin_fmaf(-mub, mub, z[3] * inv), 0.f);
    const u32 pv = pkbf(va, vb);
    const u32 pe = pkbf(blo(pv) + 1e-5f, bhi(pv) + 1e-5f);
    const u32 pd = pkbf(__builtin_amdgcn_sqrtf(blo(pe)), __builtin_amdgcn_sqrtf(bhi(pe)));
    const u32 pm = pkbf(s.ma + mua, s.mb + mub);
    s.ra = __builtin_amdgcn_rcpf(blo(pd));
    s.rb = __builtin_amdgcn_rcpf(bhi(pd));
    na = (s.ma - blo(pm)) * s.ra;
    nb = (s.mb - bhi(pm)) * s.rb;
    s.ma = blo(pm);
    s.mb = bhi(pm);
}
#define MFMA(a, b, c) __builtin_amdgcn_mfma_f32_16x16x32_bf16(a, b, c, 0, 0, 0)
#define SGB(m, n) __builtin_amdgcn_sched_group_barrier(m, n, 0)
__device__ __forceinline__ u32 ln2(u32 x, const Stat& s) {
    const u32 pd = pkbf(blo(x) - s.ma, bhi(x) - s.mb);
    return pkbf(blo(pd) * s.ra, bhi(pd) * s.rb);
}
__device__ __forceinline__ u32 add2(u32 a, u32 b) { return pkbf(blo(a) + blo(b), bhi(a) + bhi(b)); }
__device__ __forceinline__ u32 mul2(u32 a, u32 b) { return pkbf(blo(a) * blo(b), bhi(a) * bhi(b)); }
__device__ __forceinline__ v4u pair_xpose(bool odd, u32 c0a, u32 c0b, u32 c1a, u32 c1b) {
    const u32 sa = odd ? c0a : c1a, sb = odd ? c0b : c1b;
    const u32 ra = dppu<XOR1>(sa), rb = dppu<XOR1>(sb);
    return (v4u){odd ? ra : c0a, odd ? rb : c0b, odd ? c1a : ra, odd ? c1b : rb};
}

struct cf { float r, i; };
__device__ __forceinline__ cf cmul(cf a, cf b) { return {__builtin_fmaf(a.r, b.r, -(a.i * b.i)), __builtin_fmaf(a.r, b.i, a.i * b.r)}; }
template <int K> __device__ __forceinline__ cf cexpk(float la, float th, float tl) {
    const float sc = (float)(1 << K);
    const float mag = __builtin_amdgcn_exp2f(la * sc);
    const float ph = __builtin_fmaf(tl, sc, __builtin_amdgcn_fractf(th * sc));
    return {mag * __builtin_amdgcn_cosf(ph), mag * __builtin_amdgcn_sinf(ph)};
}
__device__ __forceinline__ float exp_acc(float x) {
    const float y = x * 1.44269504088896340736f;
    const float n = __builtin_rintf(y);
    float f = __builtin_fmaf(x, 1.44269504088896340736f, -n);
    f = __builtin_fmaf(x, 1.92596299e-8f, f);
    const float nc = __builtin_fminf(__builtin_fmaxf(n, -300.f), 300.f);
    const float e = __builtin_ldexpf(__builtin_amdgcn_exp2f(f), (int)nc);
    return __builtin_fabsf(y) < 400.f ? e : __builtin_amdgcn_exp2f(y);
}

#define DLY_U0 210
#define DLY_U1 260
#define DLY_Y 14
#define DLY_K 6
#define DLY_S 4
#define SSMK_POS 0
#define Q_POS 0
#define YC_POS 1
#define CPRIO 1
#define RPRIO 0
#define XPRIO 0
#define ORDER 1
#define PRE_ADV 95
#define NOROW 0
#define YL 0
#define CPF 4
#define CODE_BYTES 23936
#define HB 3
struct KG { float la, th, tl, dt; };
struct KP { u32 a, im, dt; };
__device__ __forceinline__ KP kg_load(const Params& p, int chan, int kn) {
    KP k;
    const u32 so = (u32)chan * (NST * 2);
    k.a = ldh(mkr(p.logA, DM * NST * 2), kn * 2, so);
    k.dt = ldh(mkr(p.logdt, DM * 2), 0, chan * 2);
    k.im = ldh(mkr(p.Aim, DM * NST * 2), kn * 2, so);
    return k;
}
__device__ __forceinline__ KG kg_prep(const KP& k) {
    KG g;
    g.dt = rbf(exp_acc(blo(k.dt)));
    const float zr = -rbf(exp_acc(blo(k.a))) * g.dt;
    const float zi = blo(k.im) * g.dt;
    g.la = zr * 1.44269504088896340736f;
    g.th = zi * 0.15915494309189533577f;
    g.tl = __builtin_fmaf(zi, 6.4206383e-9f, __builtin_fmaf(zi, 0.15915494309189533577f, -g.th));
    return g;
}
#define FENCE() asm volatile("" ::: "memory")
#define PUTH(row, h) do { const cf h_ = (h); *(u32*)(scr + (row) * SCR_ROW) = pkh(h_.r, -h_.i); } while (0)
#define PUTL(row, l) do { const cf l_ = (l); *(u32*)(scr + (41 + (row)) * SCR_ROW) = pkh(l_.r, l_.i); } while (0)
#define CEXP(k) cexpk<k>(g.la, g.th, g.tl)
#define PUTP(row, v) (*(u32*)(scr + (row) * SCR_ROW) = (v))
__device__ __forceinline__ u32 pkc(cf a) { return pkh(a.r, -a.i); }
__device__ __forceinline__ u32 pkp(cf a) { return pkh(a.r, a.i); }
__device__ __forceinline__ u32 hmul(u32 x, u32 y) {
    u32 t;
    asm("v_pk_mul_f16 %0, %1, %2 op_sel:[0,0] op_sel_hi:[0,1]\n\t"
        "v_pk_fma_f16 %0, %1, %2, %0 op_sel:[1,1,0] op_sel_hi:[1,0,1] neg_lo:[0,1,0]" : "=&v"(t) : "v"(x), "v"(y));
    return t;
}
#define DSK(k) (((k) & 2) ? bhi(Dsk[((k) >> 2) * 2 + ((k) & 1)]) : blo(Dsk[((k) >> 2) * 2 + ((k) & 1)]))
__device__ __forceinline__ v4u half_xpose(u32 c0a, u32 c0b, u32 c1a, u32 c1b) {
    auto sa = __builtin_amdgcn_permlane32_swap(c0a, c1a, false, false);
    auto sb = __builtin_amdgcn_permlane32_swap(c0b, c1b, false, false);
    return (v4u){sa[0], sb[0], sa[1], sb[1]};
}

__device__ __forceinline__ void get_y(rsrc_t ws, u32 off, u32 so0, u32 so1, u32 (&y)[8], int& dead) {
    v4u y0, y1;
    poll2(ws, off, so0, so1, y0, y1, dead);
    u32 o[4];
    to_rowlay(y0, o);
#pragma unroll
    for (int k = 0; k < 4; ++k) y[k] = o[k];
    to_rowlay(y1, o);
#pragma unroll
    for (int k = 0; k < 4; ++k) y[4 + k] = o[k];
}
__device__ __forceinline__ void put_u(rsrc_t ws, u32 off, u32 so, const float (&da)[8], const float (&db)[8], const Stat& s, float na, float nb) {
#pragma unroll
    for (int e = 0; e < 2; ++e) {
        u32 uh[4];
#pragma unroll
        for (int k = 0; k < 4; ++k) uh[k] = ns(pkbf(__builtin_fmaf(da[e * 4 + k], s.ra, na), __builtin_fmaf(db[e * 4 + k], s.rb, nb)));
        st16(ws, off, so + e * 262144u, from_rowlay(uh));
    }
}

__global__ void __launch_bounds__(512, 2) mega(Params p) {
    __shared__ __attribute__((aligned(16))) char smem[L_TOTAL];
    const int t = __builtin_amdgcn_workitem_id_x(), lane = t & 63;
    u32 pf;
    {
        u64 pc0;
        const u32 po = umin((u32)(t >> 6) * 3072u + umin(lane, 23) * 128u, CODE_BYTES);
        asm volatile("s_getpc_b64 %0\n\tglobal_load_dword %1, %2, %0" : "=&s"(pc0), "=&v"(pf) : "v"(po));
    }
    const int w = __builtin_amdgcn_readfirstlane(t >> 6);
    const int b = __builtin_amdgcn_workgroup_id_x();
    const int cid = (b & 7) * 32 + (b >> 3);
    const int half = (b >> (3 + HB)) & 1;
    const int rid = half * 128 + (b & 7) * 16 + ((b >> (4 + HB)) << HB) + ((b >> 3) & ((1 << HB) - 1));
    const rsrc_t ws = mkr(p.ws, WS_BYTES);
    int dead = SPIN_CAP;
    const v4u sent4 = {SENT, SENT, SENT, SENT};
#define CLR(o, so, lvl) st16(ws, o, so, sent4)
    const int li = lane & 15, lg = lane >> 4;
    const u32 sbase = (u32)(size_t)(LAS char*)smem;
    STAMP(0);
    asm volatile("" :: "s"(p.q), "s"(p.k), "s"(p.v), "s"(p.sk), "s"(p.ssmk), "s"(p.ssmD), "s"(p.logA), "s"(p.Aim), "s"(p.Cr), "s"(p.Ci), "s"(p.logdt), "s"(p.Dskip), "s"(p.out));
    u64 ep = *(const __attribute__((address_space(4))) u64*)(p.ws + WS_EPOCH);
#define BAR_EP() asm volatile("s_waitcnt lgkmcnt(0)\n\ts_barrier" : "+s"(ep) :: "memory")
    const int kch = (w >> 1) & 1, kn = (w & 1) * 64 + lane;
    char* scr = smem + L_SCR + kch * SCR_CH + kn * 4;
    const int slot = cid * 2 + kch;
    const int chan = (slot & ~63) + 2 * (slot & 31) + ((slot >> 5) & 1);
    const u32 o_w = (u32)lg * 65536u + (u32)li * 16u;
    const u32 s_w = WS_U + (u32)rid * 256u + (u32)(w & 3) * 524288u;
    const u32 o_yc = (u32)((lane >> 3) & 3) * 65536u + (u32)(lane >> 5) * 128u + (u32)(lane & 7) * 16u;
    const u32 s_yc = WS_Y + (u32)w * 262144u + (u32)cid * 256u;
    const u32 s_yw = WS_Y + (u32)(w & 1) * 131072u + (u32)slot * 128u;
#define TAG() u64 e1_ = ep; asm volatile("" : "+s"(e1_)); e1_ += 1ull; const v2u tag = {(u32)e1_, (u32)(e1_ >> 32)}
    const int r = w & 3;
    const u32 o_rw = (u32)(lane >> 5) * 4096u + (u32)(lane & 31) * 4u;
    const u32 s_rw = (u32)rid * 8192u + (u32)r * 256u;
    u32 kk[2][7], vv[2][4], Dch[2];
    v4u sk4[2];
    {
        const u32 oc = t < 256 ? o_w : 0x80000000u;
        CLR(oc, s_w, 3);
        CLR(oc, s_w + 262144u, 3);
    }
    KP kp;
    u32 pcr, pci, pcr2, pci2;
    {
        const u32 nb = w < 4 ? DM * NST * 2 : 0;
        asm volatile("buffer_load_ushort %0, %7, %9, %14 offen\n\tbuffer_load_ushort %1, off, %10, %15\n\tbuffer_load_ushort %2, %7, %11, %14 offen\n\t"
                     "buffer_load_ushort %3, %7, %12, %14 offen\n\tbuffer_load_ushort %4, %7, %13, %14 offen\n\t"
                     "buffer_load_ushort %5, %8, %12, %14 offen\n\tbuffer_load_ushort %6, %8, %13, %14 offen"
                     : "=&v"(kp.a), "=&v"(kp.dt), "=&v"(kp.im), "=&v"(pcr), "=&v"(pci), "=&v"(pcr2), "=&v"(pci2)
                     : "v"(kn * 2), "v"((kn ^ 64) * 2), "s"(mkr(p.logA, DM * NST * 2)), "s"(mkr(p.logdt, DM * 2)), "s"(mkr(p.Aim, DM * NST * 2)),
                       "s"(mkr(p.Cr, nb)), "s"(mkr(p.Ci, nb)), "s"(chan * (NST * 2)), "s"(chan * 2) : "memory");
    }
    FENCE();
    if (w >= 4) {
    {
        const rsrc_t rk = mkr(p.k, SL * DM * 2), rv = mkr(p.v, SL * DM * 2);
        const u32 ok = o_rw + s_rw - 3072u;
#pragma unroll
        for (int e = 0; e < 2; ++e) {
#pragma unroll
            for (int j = 0; j < 3; ++j) kk[e][j] = ldw(rk, ok + j * 1024u + e * 128u, 0);
#pragma unroll
            for (int j = 0; j < 4; ++j) kk[e][3 + j] = ldw(rk, o_rw + j * 1024u + e * 128u, s_rw);
#pragma unroll
            for (int j = 0; j < 4; ++j) vv[e][j] = ldw(rv, o_rw + j * 1024u + e * 128u, s_rw);
            sk4[e] = __builtin_amdgcn_raw_buffer_load_b128(mkr(p.sk, DM * 8), (lane & 31) * 16 + e * 512, r * 1024, 0);
            Dch[e] = ldh(mkr(p.ssmD, DM * 2), (lane & 31) * 4 + (lane >> 5) * 2 + e * 128, r * 256);
        }
    }
    }
    {
        u32 ia = L_U + (t >> 6) * U_CH + lane * 16;
        ia = t >= 256 ? L_RR + lane * 16 : ia;
        ia = t >= 320 ? L_CNT + li * 16 : ia;
        ia = t >= 384 ? L_T + (li >> 3) * T_CH + ((li & 7) * 258 + 257) * 16 : ia;
        ia = t >= 448 ? L_KL + (lane & 1) * KL_CH : ia;
        const u32 iv = (t >> 6) == 4 ? SENT : 0u;
        *(v4u*)(smem + ia) = (v4u){iv, iv, iv, iv};
    }
    STAMP(6);
    asm volatile("s_waitcnt lgkmcnt(0)\n\ts_barrier\n\ts_waitcnt vmcnt(26)\n\ts_cmp_lt_u32 %9, 4\n\ts_cbranch_scc0 1f\n\ts_waitcnt vmcnt(0)\n\t1:"
                 : "+s"(ep), "+v"(kp.a), "+v"(kp.dt), "+v"(kp.im), "+v"(pcr), "+v"(pci), "+v"(pcr2), "+v"(pci2), "+v"(pf) : "s"(w) : "memory", "scc");
    if (w < 4) {
        const int ch = w >> 1, par = w & 1;
        v4u sa0[2], sa1[2];
        auto late_loads = [&]() {
            FENCE();
        {
            const rsrc_t rs = mkr(p.ssmk + (size_t)chan * SL, SL * 2);
            const u32 o = (u32)(255 - (t & 127)) * 16u;
            sa1[0] = __builtin_amdgcn_raw_buffer_load_b128(rs, o, 0, 0);
            sa0[0] = __builtin_amdgcn_raw_buffer_load_b128(rs, o - 16u, 0, 0);
            sa1[1] = __builtin_amdgcn_raw_buffer_load_b128(rs, o - 2048u, 0, 0);
            sa0[1] = __builtin_amdgcn_raw_buffer_load_b128(rs, o - 2064u, 0, 0);
        }
        FENCE();
        };
        TAG();
        const u32 o_fs = lane == 0 ? WS_FLAG : 0x80000000u;
#define STFLAG(so) st8(ws, o_fs, so, tag)
        lsig(sbase + C_K1);
        STFLAG((w * 256 + cid) * 8);
        STAMP(7);
        const u32 pf1 = ldw(ws, (u32)lane * MB2 + (WS_U - 128u), 0);
        const u32 pf2 = ldw(mkr(p.out, SL * DM * 2), 0, rid * 8192);
        auto y0_clear = [&]() {
            FENCE();
            CLR(o_yc, s_yc, 2);
            CLR(o_yc, s_yc + MB1, 2);
            FENCE();
        };
        if (ORDER != 3) y0_clear();
        if (ORDER != 2 && ORDER != 4) late_loads();
        SETPRIO(CPRIO);
        {
            const KG g = kg_prep(kp);
            u32 cm = umax(umax(pcr & 0x7fffu, pci & 0x7fffu), umax(pcr2 & 0x7fffu, pci2 & 0x7fffu));
            cm = umax(cm, dppu<XOR1>(cm));
            cm = umax(cm, dppu<XOR2>(cm));
            cm = umax(cm, dppu<HMIR>(cm));
            cm = umax(cm, dppu<MIR>(cm));
            { auto sw = __builtin_amdgcn_permlane16_swap(cm, cm, false, false); cm = umax(sw[0], sw[1]); }
            { auto sw = __builtin_amdgcn_permlane32_swap(cm, cm, false, false); cm = umax(sw[0], sw[1]); }
            const int e = (int)(cm >> 7);
            const int sf = 267 - e < 253 ? 267 - e : 253;
            const float scale = u2f((u32)sf << 23);
            *(float*)(smem + L_YSC + kch * 4) = 2.f * g.dt * u2f((u32)(254 - sf) << 23);
            cf G[4], F[8];
            G[0] = {blo(pcr) * scale, blo(pci) * scale};
            const cf E7 = CEXP(7), E8 = CEXP(8), E9 = CEXP(9);
            G[1] = cmul(G[0], E7); G[2] = cmul(G[0], E8); G[3] = cmul(G[1], E8);
            F[1] = CEXP(0); F[2] = CEXP(1); F[4] = CEXP(2);
            F[3] = cmul(F[1], F[2]); F[5] = cmul(F[1], F[4]); F[6] = cmul(F[2], F[4]); F[7] = cmul(F[3], F[4]);
            u32 gp[4], fp[8];
#pragma unroll
            for (int a = 0; a < 4; ++a) gp[a] = pkc(G[a]);
#pragma unroll
            for (int a = 1; a < 8; ++a) fp[a] = pkc(F[a]);
            PUTP(0, gp[0]); PUTP(8, gp[1]); PUTP(16, gp[2]); PUTP(20, gp[3]);
            if (ORDER == 3) y0_clear();
            if (ORDER == 2) late_loads();
#pragma unroll
            for (int e2 = 1; e2 < 8; ++e2) { PUTP(e2, hmul(gp[0], fp[e2])); PUTP(8 + e2, hmul(gp[1], fp[e2])); }
#pragma unroll
            for (int e2 = 1; e2 < 4; ++e2) { PUTP(16 + e2, hmul(gp[2], fp[2 * e2])); PUTP(20 + e2, hmul(gp[3], fp[2 * e2])); }
            const u32 e9 = pkc(E9), e10 = pkc(CEXP(10));
#pragma unroll
            for (int a = 0; a < 4; ++a) {
                const u32 h = pkc(cmul(G[a], E9));
                PUTP(24 + 2 * a, h); PUTP(25 + 2 * a, hmul(h, fp[4])); PUTP(32 + a, hmul(h, e9)); PUTP(36 + a, hmul(h, e10));
            }
            PUTP(40, hmul(gp[0], pkc(CEXP(11))));
        }
        lsig(sbase + C_K);
        STAMP(1);
        const u32 o_fl1 = WS_FLAG + (u32)((cid >> 6) * 256 + (t >> 5) * 32 + (t & 1) * 16 + ((t >> 1) & 15)) * 8u;
        asm volatile("s_waitcnt vmcnt(4)" ::: "memory");
        if (ORDER != 3) STFLAG((w * 256 + cid) * 8 + 8192);
        asm volatile("" :: "v"(pf1), "v"(pf2));
        char* tslot = smem + L_T + ch * T_CH + ((t & 127) + 1) * 16;
        build_T(tslot, sa0[0], sa1[0]);
        FENCE();
        u64 fl = ldf(ws, o_fl1, 0);
        FENCE();
        build_T(tslot + 2048, sa0[1], sa1[1]);
        lsig(sbase + C_CONV);
        const int rho = li & 7;
        const u32 tb = sbase + L_T + ch * T_CH + ((rho < 4 ? 2 * rho : 2 * (rho - 4) + 1) * 258 + 2 - (li >> 3) + lg) * 16;
        const u32 ub = sbase + L_U + ch * U_CH + (4 * (li & 8) + 16 * par + 2 * (li & 7) + lg + 2) * 16;
        const u32 o_yw = (u32)(lg & 1) * 524288u + (u32)(li & 8) * 32768u + (u32)(li & 4) * 16384u + (u32)(li & 3) * 32u + (u32)(lg & 2) * 8u;
        const u32 o_ur = (u32)(t >> 1) * 256u + (u32)(t & 1) * 16u;
        const u32 s_ur = WS_U + (u32)(cid >> 3) * 65536u + (u32)(cid & 7) * 32u;
        char* uslot = smem + L_U + (t & 1) * U_CH + (64 + (t >> 1)) * 16;
        u32 tq = (u32)__builtin_amdgcn_s_memrealtime();
        STAMP(26);
        lwait(smem + C_CONV, 4);
        STAMP(2);
        asm volatile(TLOAD_ASM :: "v"(tb) : "memory", TLOAD_CLOB);
        pollf(ws, o_fl1, 0, fl, e1_, dead);
        if (NOROW) __builtin_amdgcn_s_sleep(NOROW);
        v4u uin;
        u32 tg = 8 * LINC + 1u;
#define PUT_Y(so) do { \
            a0 += a1; a2 += a3; \
            auto s0 = __builtin_amdgcn_permlane16_swap(ns(pkbf(a0[0], a0[1])), ns(pkbf(a2[0], a2[1])), false, false); \
            auto s1 = __builtin_amdgcn_permlane16_swap(ns(pkbf(a0[2], a0[3])), ns(pkbf(a2[2], a2[3])), false, false); \
            st16(ws, o_yw, so, (v4u){s0[0], s1[0], s0[1], s1[1]}); \
            lsig(sbase + C_Y); } while (0)
#define TRI(uu) do { u32 t1_ = tg, t2_; \
            asm volatile(TRI_ASM : "+v"(a0), "+v"(a1), "+v"(a2), "+v"(a3), "+s"(t1_), "=&s"(t2_) : "v"(sbase + C_CONV), "v"(uu) : "memory", "scc", CONV_CLOB); \
            tg += 4 * LINC; } while (0)
#pragma unroll 1
        for (int L = 0;; ++L) {
            const int uo = (L & 1) * 2 * U_CH;
            const u32 lo = (u32)L * MB2;
            const u32 u = ub + uo;
            if (L) STAMP(3 + L * 8);
            poll1(ws, o_ur, s_ur + lo, uin, dead);
            *(v4u*)(uslot + uo) = uin;
            lsig(sbase + C_CONV);
            u64 tn = __builtin_amdgcn_s_memrealtime();
            const u32 oob = L == 2 ? 0x80000000u : 0u;
            CLR(o_w | oob, s_w + lo + MB2, 1);
            CLR(o_w | oob, s_w + lo + MB2 + 262144u, 1);
            v4f a0 = {0.f, 0.f, 0.f, 0.f}, a1 = a0, a2 = a0, a3 = a0;
            STAMP(4 + L * 8);
            TRI(u);
            PUT_Y(s_yw + lo);
            STAMP(5 + L * 8);
            CLR(o_yc | oob, s_yc + lo + MB2, 1);
            CLR(o_yc | oob, s_yc + lo + MB2 + MB1, 1);
            a0 = (v4f){0.f, 0.f, 0.f, 0.f}; a1 = a0; a2 = a0; a3 = a0;
            asm volatile("" : "+s"(tn));
            const bool pre = (int)((u32)tn + PRE_ADV - tq) >= 0;
            asm volatile(CROSS_ASM : "+v"(a0), "+v"(a1), "+v"(a2), "+v"(a3), "=&v"(uin)
                         : "v"(u), "v"(tb), "v"(pre ? o_ur : 0x80000000u), "s"(ws), "s"(s_ur + lo + 32768u) : "memory", CONV_CLOB);
            lsig(sbase + C_CL);
            STAMP(6 + L * 8);
            if (!pre || __builtin_amdgcn_ballot_w64(!isvalid(uin)) != 0) {
                u32 n = 0;
#pragma nounroll
                while ((int)((u32)__builtin_amdgcn_s_memrealtime() - tq) < 0 && ++n < 1000) __builtin_amdgcn_s_sleep(1);
                poll1(ws, o_ur, s_ur + lo + 32768u, uin, dead);
            }
            *(v4u*)(uslot + uo + 2048) = uin;
            lsig(sbase + C_CONV);
            STFLAG((w * 256 + cid) * 8 + 16384 + L * 8192);
            STAMP(8 + L * 8);
            TRI(u + 2048);
            PUT_Y(s_yw + lo + MB1);
            STAMP(9 + L * 8);
            if (L == 2) break;
            tq = (u32)__builtin_amdgcn_s_memrealtime() + (L == 0 ? DLY_U0 : DLY_U1);
            if (L == 0) {
                lwait(smem + C_ROW, 12);
                STAMP(10);
                const char* kl = smem + L_KL + ch * KL_CH + (127 - (t & 127)) * 16;
                build_T(tslot, *(const v4u*)(kl + 2048), *(const v4u*)(kl + 2064));
                build_T(tslot + 2048, *(const v4u*)kl, *(const v4u*)(kl + 16));
                lsig(sbase + C_CONV);
                tg += 4 * LINC;
                lwait(smem + C_CONV, 16);
                asm volatile(TLOAD_ASM :: "v"(tb) : "memory", TLOAD_CLOB);
                STAMP(27);
            }
            fl = ldf(ws, o_fl1, 16384 + L * 8192);
            pollf(ws, o_fl1, 16384 + L * 8192, fl, e1_, dead);
            lwait(smem + C_RY, 4 * L + 4);
        }
        STAMP_OUT();
        return;
    }
    SETPRIO(RPRIO);
    {
        const KG g = kg_prep(kp);
        cf L[9];
        L[1] = CEXP(3); L[2] = CEXP(4); L[4] = CEXP(5); L[8] = CEXP(6);
        L[3] = cmul(L[1], L[2]); L[5] = cmul(L[1], L[4]); L[6] = cmul(L[2], L[4]); L[7] = cmul(L[3], L[4]);
        PUTP(41, 0x3c00u);
        const u32 l8 = pkp(L[8]);
#pragma unroll
        for (int i = 1; i < 8; ++i) { const u32 l = pkp(L[i]); PUTP(41 + i, l); PUTP(49 + i, hmul(l, l8)); }
        PUTP(49, l8);
    }
    lsig(sbase + C_K);
    asm volatile("" : "+v"(kk[0][0]), "+v"(kk[0][1]), "+v"(kk[0][2]), "+v"(kk[0][3]), "+v"(kk[0][4]), "+v"(kk[0][5]), "+v"(kk[0][6]), "+v"(sk4[0]) :: "memory");
    SETPRIO(3);
    STAMP(20);
    u32 qq[2][4], pil, Dsk[4];
    auto q_loads = [&]() {
    {
        const rsrc_t rq = mkr(p.q, SL * DM * 2);
#pragma unroll
        for (int e = 0; e < 2; ++e) {
#pragma unroll
            for (int j = 0; j < 4; ++j) qq[e][j] = ldw(rq, o_rw + j * 1024u + e * 128u, s_rw);
        }
        pil = ldh(rq, lg * 2048, rid * 8192) | (ldh(rq, lg * 2048 + 1024, rid * 8192) << 16);
    }
    {
        const rsrc_t rd2 = mkr(p.Dskip, DM * 2);
#pragma unroll
        for (int k = 0; k < 4; ++k) Dsk[k] = ldw(rd2, li * 4 + k * 64, r * 256);
    }
        FENCE();
    };
    if (Q_POS == 0) q_loads();
    FENCE();
    v4u kvp[2];
    {
#pragma unroll
        for (int e = 0; e < 2; ++e) {
            const float w00 = blo(sk4[e].x), w01 = bhi(sk4[e].x), w02 = blo(sk4[e].y), w03 = bhi(sk4[e].y);
            const float w10 = blo(sk4[e].z), w11 = bhi(sk4[e].z), w12 = blo(sk4[e].w), w13 = bhi(sk4[e].w);
            float a0[4], a1[4];
#pragma unroll
            for (int j = 0; j < 4; ++j) {
                const float s0 = __builtin_fmaf(blo(kk[e][j + 3]), w00, __builtin_fmaf(blo(kk[e][j + 2]), w01, __builtin_fmaf(blo(kk[e][j + 1]), w02, blo(kk[e][j]) * w03)));
                const float s1 = __builtin_fmaf(bhi(kk[e][j + 3]), w10, __builtin_fmaf(bhi(kk[e][j + 2]), w11, __builtin_fmaf(bhi(kk[e][j + 1]), w12, bhi(kk[e][j]) * w13)));
                a0[j] = s0 * blo(vv[e][j]);
                a1[j] = s1 * bhi(vv[e][j]);
            }
            kvp[e] = half_xpose(ns(pkbf(a0[0], a0[1])), ns(pkbf(a0[2], a0[3])), ns(pkbf(a1[0], a1[1])), ns(pkbf(a1[2], a1[3])));
            if (e == 0) lwait(smem + C_K1, 4);
            st16(ws, o_w, s_w + e * 262144u, kvp[e]);
        }
    }
    STAMP(1);
    SETPRIO(XPRIO);
    lsig(sbase + C_ROW);
    if (Q_POS == 1) q_loads();
    int io = (r & 1) == 0 ? 128 * (lg >> 1) + 4 * (lg & 1) + 8 * li : 256 + 64 * lg + (lg < 2 ? 4 : 2) * li;
    int st2 = (r & 1) && lg >= 2 ? 32 : 2;
    pin(io); pin(st2);
    lwait(smem + C_K, 8);
    STAMP(2);
    {
        const char* bb = smem + L_SCR + kch * SCR_CH + (41 + li) * SCR_ROW + lg * 16;
        const char* ab = smem + L_SCR + kch * SCR_CH + ((r & 1) * 16 + li) * SCR_ROW + lg * 16;
        const float ysc = *(const float*)(smem + L_YSC + kch * 4);
        v4f c0 = {0.f, 0.f, 0.f, 0.f}, c1 = c0;
#pragma unroll
        for (int ks = 0; ks < 8; ++ks) {
            const v8h bf = *(const v8h*)(bb + ks * 64);
            c0 = __builtin_amdgcn_mfma_f32_16x16x32_f16(*(const v8h*)(ab + ks * 64), bf, c0, 0, 0, 0);
            c1 = __builtin_amdgcn_mfma_f32_16x16x32_f16(*(const v8h*)(bb - 9 * SCR_ROW + ks * 64), bf, c1, 0, 0, 0);
        }
        SGB(0x100, 10);
#pragma unroll
        for (int ks = 0; ks < 5; ++ks) { SGB(0x8, 2); SGB(0x100, 3); }
        SGB(0x8, 6);
        char* tab = smem + L_KTAB + kch * 1536;
        asm volatile("s_nop 7\n\ts_nop 2" : "+a"(c0), "+a"(c1));
        *(u32*)(tab + io * 2) = pkbf(c0[0] * ysc, c0[1] * ysc);
        *(u32*)(tab + (io + st2) * 2) = pkbf(c0[2] * ysc, c0[3] * ysc);
        const u32 p01 = pkbf(c1[0] * ysc, c1[1] * ysc), p23 = pkbf(c1[2] * ysc, c1[3] * ysc);
        u16* t2 = (u16*)tab + 512 + 64 * lg + li;
        t2[0] = (u16)p01; t2[16] = (u16)(p01 >> 16); t2[32] = (u16)p23; t2[48] = (u16)(p23 >> 16);
    }
    lsig(sbase + C_ROW);
    STAMP(3);
    const u32 o_fl = WS_FLAG + (u32)((cid >> 6) * 256 + (t & 255)) * 8u;
    u32 x[8], yd[8];
    u64 fl;
    Stat s;
    float ga[8], gb[8], ca[8], cb[8];
    {
#pragma unroll
        for (int e = 0; e < 2; ++e) {
            pin(Dch[e]);
            const float d0 = blo(Dch[e]);
            u32 o[4];
            to_rowlay((v4u){pkbf(blo(kvp[e].x) * d0, bhi(kvp[e].x) * d0), pkbf(blo(kvp[e].y) * d0, bhi(kvp[e].y) * d0),
                            pkbf(blo(kvp[e].z) * d0, bhi(kvp[e].z) * d0), pkbf(blo(kvp[e].w) * d0, bhi(kvp[e].w) * d0)}, o);
#pragma unroll
            for (int k = 0; k < 4; ++k) yd[e * 4 + k] = o[k];
        }
#pragma unroll
        for (int k = 0; k < 8; ++k) pin(yd[k]);
#pragma unroll
        for (int e = 0; e < 2; ++e) {
            u32 o[4];
            to_rowlay(half_xpose(__builtin_amdgcn_perm(qq[e][1], qq[e][0], 0x05040100u), __builtin_amdgcn_perm(qq[e][3], qq[e][2], 0x05040100u),
                                 __builtin_amdgcn_perm(qq[e][1], qq[e][0], 0x07060302u), __builtin_amdgcn_perm(qq[e][3], qq[e][2], 0x07060302u)), o);
#pragma unroll
            for (int k = 0; k < 4; ++k) x[e * 4 + k] = o[k];
        }
        s.ma = blo(pil); s.mb = bhi(pil);
        float da[8], db[8], na, nb;
#pragma unroll
        for (int k = 0; k < 8; ++k) { da[k] = blo(x[k]) - s.ma; db[k] = bhi(x[k]) - s.mb; }
        row_stats(smem + L_RR, r, lane, da, db, s, na, nb);
#pragma unroll
        for (int k = 0; k < 8; ++k) {
            const float ea = blo(x[k]) - s.ma, eb = bhi(x[k]) - s.mb;
            const u32 pd = pkbf(ea, eb);
            const u32 n0 = pkbf(blo(pd) * s.ra, bhi(pd) * s.rb);
            ga[k] = blo(n0); gb[k] = bhi(n0);
            ca[k] = __builtin_fmaf(ga[k], blo(yd[k]), ea);
            cb[k] = __builtin_fmaf(gb[k], bhi(yd[k]), eb);
            pin(ca[k]); pin(cb[k]);
        }
    }
    STAMP(7);
    fl = ldf(ws, o_fl, 8192);
    auto taps = [&]() {
    lwait(smem + C_ROW, 8);
    {
        const int c = 255 - (t & 255);
        v4u kc[2];
        if (r < 2) {
#pragma unroll
            for (int e = 0; e < 2; ++e) {
                const char* tab = smem + L_KTAB + e * 1536;
                const u32 a = *(const u16*)(tab + (384 + c) * 2), bq = *(const u16*)(tab + (385 + c) * 2);
                const u32 aa = a | (a << 16), bb = bq | (bq << 16);
                kc[e] = (v4u){aa, aa, (c & 1) ? bb : (a | (bq << 16)), bb};
            }
        } else if (r == 2) {
#pragma unroll
            for (int e = 0; e < 2; ++e) {
                const char* tab = smem + L_KTAB + e * 1536;
                const u32 d0 = *(const u32*)(tab + (256 + 2 * c) * 2), d1 = *(const u32*)(tab + (258 + 2 * c) * 2);
                kc[e] = (v4u){__builtin_amdgcn_perm(d0, d0, 0x01000100u), d0, __builtin_amdgcn_perm(d0, d0, 0x03020302u), __builtin_amdgcn_perm(d1, d1, 0x01000100u)};
            }
        } else {
            const bool s1 = c >= 32;
#pragma unroll
            for (int e = 0; e < 2; ++e) {
                const char* a = smem + L_KTAB + e * 1536 + (s1 ? (128 + 4 * c) * 2 : 16 * c);
                const v2u d01 = *(const v2u*)a, d23 = *(const v2u*)(a + 8);
                kc[e].x = s1 ? __builtin_amdgcn_perm(d01.x, d01.x, 0x01000100u) : d01.x;
                kc[e].y = s1 ? __builtin_amdgcn_alignbit(d01.y, d01.x, 16) : d01.y;
                kc[e].z = s1 ? __builtin_amdgcn_perm(d01.y, d01.y, 0x01000100u) : d23.x;
                kc[e].w = s1 ? __builtin_amdgcn_alignbit(d23.x, d01.y, 16) : d23.y;
            }
        }
        *(v4u*)(smem + L_KL + (c + 1) * 16) = kc[0];
        *(v4u*)(smem + L_KL + KL_CH + (c + 1) * 16) = kc[1];
    }
    lsig(sbase + C_ROW);
    };
    const bool late = lpeek(smem + (half ? C_Y : C_CL), 4 + 4 * half);
    if (!late) taps();
    STAMP(4);
    TAG();
    pollf(ws, o_fl, 8192, fl, e1_, dead);
    STAMP(5);
    SETPRIO(3);
    const u32 o_yr = (u32)lane * 128u;
    const u32 s_yr = WS_Y + (u32)(rid >> 3) * 65536u + (u32)(rid & 7) * 16u + (u32)r * 16384u;
    u32 y[8];
    float da[8], db[8], na, nb;
    if (!lpeek(smem + C_Y, 4 * half + 4)) {
        lwait(smem + C_Y, 4 * half + 4);
        __builtin_amdgcn_s_sleep(DLY_Y);
    }
    STAMP(8);
    get_y(ws, o_yr, s_yr, s_yr + (YL ? 4096u : 8192u), y, dead);
    const u32 c_m1 = sbase + (half ? C_RY : C_DUM), c_m2 = sbase + (half ? C_DUM : C_RY);
    lsig(c_m1);
    lwait(smem + C_CL, 4);
    STAMP(9);
#pragma unroll
    for (int k = 0; k < 8; ++k) {
        da[k] = __builtin_fmaf(ga[k], blo(y[k]), ca[k]);
        db[k] = __builtin_fmaf(gb[k], bhi(y[k]), cb[k]);
    }
    row_stats(smem + L_RR + 256, r, lane, da, db, s, na, nb);
    put_u(ws, o_w, s_w + MB2, da, db, s, na, nb);
    STAMP(11);
    if (late) taps();
    SETPRIO(0);
#pragma unroll
    for (int k = 0; k < 8; ++k) {
        const u32 z = add2(y[k], yd[k]);
        x[k] = add2(x[k], pkbf(ga[k] * blo(z), gb[k] * bhi(z)));
        ga[k] = blo(z); gb[k] = bhi(z);
        const float ea = blo(x[k]) - s.ma, eb = bhi(x[k]) - s.mb;
        const u32 pd = pkbf(ea, eb);
        const u32 u = pkbf(blo(pd) * s.ra, bhi(pd) * s.rb);
        const float dk = DSK(k);
        yd[k] = pkbf(blo(u) * dk, bhi(u) * dk);
        ca[k] = ea + blo(yd[k]);
        cb[k] = eb + bhi(yd[k]);
        pin(ca[k]); pin(cb[k]); pin(ga[k]); pin(gb[k]);
    }
    wait_vm0();
    lsig(c_m2);
    if (b == 0 && t == 256) st8(ws, WS_EPOCH, 0, tag);
    SETPRIO(3);
    STAMP(12);
    if (!lpeek(smem + C_Y, 4 * half + 12)) {
        lwait(smem + C_Y, 4 * half + 12);
        __builtin_amdgcn_s_sleep(DLY_Y);
    }
    STAMP(15);
    get_y(ws, o_yr, s_yr + MB2, s_yr + MB2 + (YL ? 4096u : 8192u), y, dead);
    lsig(c_m1);
    lwait(smem + C_CL, 8);
    STAMP(16);
#pragma unroll
    for (int k = 0; k < 8; ++k) { da[k] = blo(y[k]) + ca[k]; db[k] = bhi(y[k]) + cb[k]; }
    Stat s2 = s;
    row_stats(smem + L_RR + 512, r, lane, da, db, s2, na, nb);
#pragma unroll
    for (int k = 0; k < 8; ++k) {
        da[k] = __builtin_fmaf(__builtin_fmaf(da[k], s2.ra, na), ga[k], da[k]);
        db[k] = __builtin_fmaf(__builtin_fmaf(db[k], s2.rb, nb), gb[k], db[k]);
    }
    STAMP(17);
    row_stats(smem + L_RR + 768, r, lane, da, db, s, na, nb);
    put_u(ws, o_w, s_w + 2 * MB2, da, db, s, na, nb);
    STAMP(18);
    SETPRIO(0);
    {
        char* tr = smem + L_SCR + r * 4352;
#pragma unroll
        for (int k = 0; k < 8; ++k) {
            u32 xx = add2(x[k], add2(y[k], yd[k]));
            const u32 n = ln2(xx, s2);
            xx = add2(xx, pkbf(blo(n) * ga[k], bhi(n) * gb[k]));
            const u32 u = ln2(xx, s);
            const float dk = DSK(k);
            const u32 d = pkbf(blo(u) * dk, bhi(u) * dk);
            const int co = (64 * (k >> 2) + 32 * (k & 1) + ((k >> 1) & 1)) * 4;
            *(float*)(tr + lg * 1088 + li * 8 + co) = blo(xx) + blo(d);
            *(float*)(tr + lg * 1088 + 544 + li * 8 + co) = bhi(xx) + bhi(d);
        }
        wait_vm0();
        lsig(c_m2);
#pragma unroll
        for (int e = 0; e < 2; ++e) {
            const v4f c0 = *(const v4f*)(tr + lg * 1088 + li * 32 + e * 16), c1 = *(const v4f*)(tr + lg * 1088 + 544 + li * 32 + e * 16);
#pragma unroll
            for (int k = 0; k < 4; ++k) { ca[e * 4 + k] = c0[k]; cb[e * 4 + k] = c1[k]; pin(ca[e * 4 + k]); pin(cb[e * 4 + k]); }
        }
    }
    SETPRIO(3);
    STAMP(19);
    if (!lpeek(smem + C_Y, 4 * half + 20)) {
        lwait(smem + C_Y, 4 * half + 20);
        __builtin_amdgcn_s_sleep(DLY_Y);
    }
    STAMP(22);
    get_y(ws, (u32)(li & 8) * 1024u + (u32)(li & 7) * 512u + (u32)(lg >> 1) * 128u + (u32)(lg & 1) * 4096u, s_yr + 2 * MB2, s_yr + 2 * MB2 + 256u, y, dead);
    STAMP(23);
    {
        const rsrc_t ro = mkr(p.out, SL * DM * 2);
        const u32 oo = (u32)lg * 2048u + (u32)li * 16u;
        v4u o0, o1;
#pragma unroll
        for (int k = 0; k < 4; ++k) {
            o0[k] = pkbf(blo(y[2 * k]) + ca[2 * k], blo(y[2 * k + 1]) + ca[2 * k + 1]);
            o1[k] = pkbf(bhi(y[2 * k]) + cb[2 * k], bhi(y[2 * k + 1]) + cb[2 * k + 1]);
        }
        st16(ro, oo, s_rw, o0);
        st16(ro, oo + 1024u, s_rw, o1);
    }
    STAMP(28);
}
#if defined(__HIP_DEVICE_COMPILE__)
#pragma clang attribute pop
#endif

extern "C" void kernel_launch(void* const* d_in, const int* in_sizes, int n_in,
                              void* d_out, int out_size, void* d_ws, size_t ws_size,
                              hipStream_t stream) {
    Params p;
    p.q = (const u16*)d_in[0];
    p.k = (const u16*)d_in[1];
    p.v = (const u16*)d_in[2];
    p.sk = (const u16*)d_in[3];
    p.ssmk = (const u16*)d_in[4];
    p.ssmD = (const u16*)d_in[5];
    p.logA = (const u16*)d_in[7];
    p.Aim = (const u16*)d_in[8];
    p.Cr = (const u16*)d_in[9];
    p.Ci = (const u16*)d_in[10];
    p.logdt = (const u16*)d_in[11];
    p.Dskip = (const u16*)d_in[12];
    p.out = (u16*)d_out;
    p.ws = (char*)d_ws;
    mega<<<256, 512, 0, stream>>>(p);
}
```

```cpp
#if defined(__HIP_DEVICE_COMPILE__)
#pragma clang attribute push(__attribute__((target("no-packed-fp32-ops"))), apply_to = function)
#endif
#include <hip/hip_runtime.h>
#include <stdint.h>

typedef unsigned short u16;
typedef unsigned u32;
typedef unsigned long long u64;
typedef u32 v4u __attribute__((ext_vector_type(4)));
typedef u32 v2u __attribute__((ext_vector_type(2)));
typedef float v4f __attribute__((ext_vector_type(4)));
typedef float v2f __attribute__((ext_vector_type(2)));
typedef __bf16 v2bf __attribute__((ext_vector_type(2)));
typedef __bf16 v8bf __attribute__((ext_vector_type(8)));
typedef _Float16 v8h __attribute__((ext_vector_type(8)));
typedef _Float16 v2h __attribute__((ext_vector_type(2)));
#define GAS __attribute__((address_space(1)))
#define LAS __attribute__((address_space(3)))
typedef GAS u64 gu64;
#define RLX_AGENT __ATOMIC_RELAXED, __HIP_MEMORY_SCOPE_AGENT

constexpr int SL = 2048, DM = 512, NST = 128;
constexpr u32 SENT = 0xFFFFFFFFu;
constexpr u32 SPIN_CAP = 1u << 15;

constexpr u32 WS_EPOCH = 0;
constexpr u32 WS_FLAG = 4096;
constexpr u32 MB1 = 1u << 20;
constexpr u32 MB2 = (2u << 20) + 65536u;
constexpr u32 WS_U = 65536u;
constexpr u32 WS_Y = WS_U + 3u * MB2;
constexpr u32 WS_BYTES = WS_U + 6u * MB2;
constexpr u32 LSPIN_CAP = 1u << 20;

constexpr int L_RR = 0;
constexpr int L_CNT = 1024;
constexpr int C_CONV = L_CNT, C_ROW = L_CNT + 64, C_Y = L_CNT + 128, C_K = L_CNT + 192, C_K1 = L_CNT + 224, L_YSC = L_CNT + 240, C_RY = L_CNT + 96, C_DUM = L_CNT + 160, C_CL = L_CNT + 32;
constexpr u32 LINC = 1u << 17;
constexpr int T_CH = 8 * 258 * 16;
constexpr int L_T = 1280;
constexpr int SCR_ROW = 528;
constexpr int SCR_CH = 57 * SCR_ROW;
constexpr int L_SCR = L_T + 2 * T_CH;
constexpr int U_CH = 320 * 16;
constexpr int L_U = L_SCR + 2 * SCR_CH;
constexpr int L_KTAB = L_U + 4 * U_CH;
constexpr int L_KL = L_KTAB + 2 * 1536;
constexpr int KL_CH = 257 * 16;
constexpr int L_TOTAL0 = L_KL + 2 * KL_CH;

constexpr int L_TOTAL = L_TOTAL0;
#define STAMP(i) do {} while (0)
#define STAMP_OUT() do {} while (0)
static_assert(L_TOTAL <= 163840, "lds");
struct Params {
    const u16 *q, *k, *v, *sk, *ssmk, *ssmD, *logA, *Aim, *Cr, *Ci, *logdt, *Dskip;
    u16* out;
    char* ws;
};

#define TLOAD_ASM \
    "ds_read_b128 a[0:3], %0 offset:4032\n\t" \
    "ds_read_b128 a[32:35], %0 offset:3520\n\t" \
    "ds_read_b128 a[64:67], %0 offset:3008\n\t" \
    "ds_read_b128 a[96:99], %0 offset:2496\n\t" \
    "ds_read_b128 a[4:7], %0 offset:3968\n\t" \
    "ds_read_b128 a[36:39], %0 offset:3456\n\t" \
    "ds_read_b128 a[68:71], %0 offset:2944\n\t" \
    "ds_read_b128 a[100:103], %0 offset:2432\n\t" \
    "ds_read_b128 a[8:11], %0 offset:3904\n\t" \
    "ds_read_b128 a[40:43], %0 offset:3392\n\t" \
    "ds_read_b128 a[72:75], %0 offset:2880\n\t" \
    "ds_read_b128 a[104:107], %0 offset:2368\n\t" \
    "ds_read_b128 a[12:15], %0 offset:3840\n\t" \
    "ds_read_b128 a[44:47], %0 offset:3328\n\t" \
    "ds_read_b128 a[76:79], %0 offset:2816\n\t" \
    "ds_read_b128 a[108:111], %0 offset:2304\n\t" \
    "ds_read_b128 a[16:19], %0 offset:3776\n\t" \
    "ds_read_b128 a[48:51], %0 offset:3264\n\t" \
    "ds_read_b128 a[80:83], %0 offset:2752\n\t" \
    "ds_read_b128 a[112:115], %0 offset:2240\n\t" \
    "ds_read_b128 a[20:23], %0 offset:3712\n\t" \
    "ds_read_b128 a[52:55], %0 offset:3200\n\t" \
    "ds_read_b128 a[84:87], %0 offset:2688\n\t" \
    "ds_read_b128 a[116:119], %0 offset:2176\n\t" \
    "ds_read_b128 a[24:27], %0 offset:3648\n\t" \
    "ds_read_b128 a[56:59], %0 offset:3136\n\t" \
    "ds_read_b128 a[88:91], %0 offset:2624\n\t" \
    "ds_read_b128 a[120:123], %0 offset:2112\n\t" \
    "ds_read_b128 a[28:31], %0 offset:3584\n\t" \
    "ds_read_b128 a[60:63], %0 offset:3072\n\t" \
    "ds_read_b128 a[92:95], %0 offset:2560\n\t" \
    "ds_read_b128 a[124:127], %0 offset:2048\n\t"
#define TLOAD_CLOB "a0", "a1", "a2", "a3", "a4", "a5", "a6", "a7", "a8", "a9", "a10", "a11", "a12", "a13", "a14", "a15", "a16", "a17", "a18", "a19", "a20", "a21", "a22", "a23", "a24", "a25", "a26", "a27", "a28", "a29", "a30", "a31", "a32", "a33", "a34", "a35", "a36", "a37", "a38", "a39", "a40", "a41", "a42", "a43", "a44", "a45", "a46", "a47", "a48", "a49", "a50", "a51", "a52", "a53", "a54", "a55", "a56", "a57", "a58", "a59", "a60", "a61", "a62", "a63", "a64", "a65", "a66", "a67", "a68", "a69", "a70", "a71", "a72", "a73", "a74", "a75", "a76", "a77", "a78", "a79", "a80", "a81", "a82", "a83", "a84", "a85", "a86", "a87", "a88", "a89", "a90", "a91", "a92", "a93", "a94", "a95", "a96", "a97", "a98", "a99", "a100", "a101", "a102", "a103", "a104", "a105", "a106", "a107", "a108", "a109", "a110", "a111", "a112", "a113", "a114", "a115", "a116", "a117", "a118", "a119", "a120", "a121", "a122", "a123", "a124", "a125", "a126", "a127"
#define CONV_CLOB "v64", "v65", "v66", "v67", "v68", "v69", "v70", "v71", "v72", "v73", "v74", "v75", "v76", "v77", "v78", "v79", "v80", "v81", "v82", "v83", "v84", "v85", "v86", "v87", "v88", "v89", "v90", "v91", "v92", "v93", "v94", "v95", "v96", "v97", "v98", "v99", "v100", "v101", "v102", "v103", "v104", "v105", "v106", "v107", "v108", "v109", "v110", "v111", "v112", "v113", "v114", "v115", "v116", "v117", "v118", "v119", "v120", "v121", "v122", "v123", "v124", "v125", "v126", "v127"
#define TRI_ASM \
    "1:\n\t" \
    "s_sub_u32 %4, %4, 1\n\t" \
    "ds_read_b32 v112, %6\n\t" \
    "ds_read_b128 v[64:67], %7 offset:1984\n\t" \
    "ds_read_b128 v[68:71], %7 offset:1472\n\t" \
    "ds_read_b128 v[72:75], %7 offset:960\n\t" \
    "ds_read_b128 v[76:79], %7 offset:448\n\t" \
    "ds_read_b128 v[80:83], %7 offset:1920\n\t" \
    "ds_read_b128 v[84:87], %7 offset:1408\n\t" \
    "ds_read_b128 v[88:91], %7 offset:896\n\t" \
    "ds_read_b128 v[92:95], %7 offset:384\n\t" \
    "ds_read_b128 v[96:99], %7 offset:1856\n\t" \
    "ds_read_b128 v[100:103], %7 offset:1344\n\t" \
    "ds_read_b128 v[104:107], %7 offset:832\n\t" \
    "ds_read_b128 v[108:111], %7 offset:320\n\t" \
    "s_waitcnt lgkmcnt(12)\n\t" \
    "v_readfirstlane_b32 %5, v112\n\t" \
    "s_cmp_lt_u32 %5, %4\n\t" \
    "s_cbranch_scc1 1b\n\t" \
    "s_waitcnt lgkmcnt(11)\n\t" \
    "v_mfma_f32_16x16x32_bf16 %2, a[0:3], v[64:67], %2\n\t" \
    "ds_read_b128 v[112:115], %7 offset:1792\n\t" \
    "s_waitcnt lgkmcnt(11)\n\t" \
    "v_mfma_f32_16x16x32_bf16 %3, a[32:35], v[68:71], %3\n\t" \
    "ds_read_b128 v[116:119], %7 offset:1280\n\t" \
    "s_waitcnt lgkmcnt(11)\n\t" \
    "v_mfma_f32_16x16x32_bf16 %0, a[0:3], v[72:75], %0\n\t" \
    "ds_read_b128 v[120:123], %7 offset:768\n\t" \
    "v_mfma_f32_16x16x32_bf16 %2, a[64:67], v[72:75], %2\n\t" \
    "ds_read_b128 v[124:127], %7 offset:256\n\t" \
    "s_waitcnt lgkmcnt(12)\n\t" \
    "v_mfma_f32_16x16x32_bf16 %1, a[32:35], v[76:79], %1\n\t" \
    "v_mfma_f32_16x16x32_bf16 %3, a[96:99], v[76:79], %3\n\t" \
    "s_waitcnt lgkmcnt(10)\n\t" \
    "v_mfma_f32_16x16x32_bf16 %2, a[4:7], v[80:83], %2\n\t" \
    "ds_read_b128 v[64:67], %7 offset:1728\n\t" \
    "v_mfma_f32_16x16x32_bf16 %3, a[36:39], v[84:87], %3\n\t" \
    "ds_read_b128 v[68:71], %7 offset:1216\n\t" \
    "s_waitcnt lgkmcnt(10)\n\t" \
    "v_mfma_f32_16x16x32_bf16 %0, a[4:7], v[88:91], %0\n\t" \
    "ds_read_b128 v[72:75], %7 offset:704\n\t" \
    "v_mfma_f32_16x16x32_bf16 %2, a[68:71], v[88:91], %2\n\t" \
    "ds_read_b128 v[76:79], %7 offset:192\n\t" \
    "v_mfma_f32_16x16x32_bf16 %1, a[36:39], v[92:95], %1\n\t" \
    "v_mfma_f32_16x16x32_bf16 %3, a[100:103], v[92:95], %3\n\t" \
    "s_waitcnt lgkmcnt(10)\n\t" \
    "v_mfma_f32_16x16x32_bf16 %2, a[8:11], v[96:99], %2\n\t" \
    "ds_read_b128 v[80:83], %7 offset:1664\n\t" \
    "v_mfma_f32_16x16x32_bf16 %3, a[40:43], v[100:103], %3\n\t" \
    "ds_read_b128 v[84:87], %7 offset:1152\n\t" \
    "s_waitcnt lgkmcnt(10)\n\t" \
    "v_mfma_f32_16x16x32_bf16 %0, a[8:11], v[104:107], %0\n\t" \
    "ds_read_b128 v[88:91], %7 offset:640\n\t" \
    "v_mfma_f32_16x16x32_bf16 %2, a[72:75], v[104:107], %2\n\t" \
    "ds_read_b128 v[92:95], %7 offset:128\n\t" \
    "v_mfma_f32_16x16x32_bf16 %1, a[40:43], v[108:111], %1\n\t" \
    "v_mfma_f32_16x16x32_bf16 %3, a[104:107], v[108:111], %3\n\t" \
    "s_waitcnt lgkmcnt(10)\n\t" \
    "v_mfma_f32_16x16x32_bf16 %2, a[12:15], v[112:115], %2\n\t" \
    "ds_read_b128 v[96:99], %7 offset:1600\n\t" \
    "v_mfma_f32_16x16x32_bf16 %3, a[44:47], v[116:119], %3\n\t" \
    "ds_read_b128 v[100:103], %7 offset:1088\n\t" \
    "s_waitcnt lgkmcnt(10)\n\t" \
    "v_mfma_f32_16x16x32_bf16 %0, a[12:15], v[120:123], %0\n\t" \
    "ds_read_b128 v[104:107], %7 offset:576\n\t" \
    "v_mfma_f32_16x16x32_bf16 %2, a[76:79], v[120:123], %2\n\t" \
    "ds_read_b128 v[108:111], %7 offset:64\n\t" \
    "v_mfma_f32_16x16x32_bf16 %1, a[44:47], v[124:127], %1\n\t" \
    "v_mfma_f32_16x16x32_bf16 %3, a[108:111], v[124:127], %3\n\t" \
    "s_waitcnt lgkmcnt(10)\n\t" \
    "v_mfma_f32_16x16x32_bf16 %2, a[16:19], v[64:67], %2\n\t" \
    "ds_read_b128 v[112:115], %7 offset:1536\n\t" \
    "v_mfma_f32_16x16x32_bf16 %3, a[48:51], v[68:71], %3\n\t" \
    "ds_read_b128 v[116:119], %7 offset:1024\n\t" \
    "s_waitcnt lgkmcnt(10)\n\t" \
    "v_mfma_f32_16x16x32_bf16 %0, a[16:19], v[72:75], %0\n\t" \
    "ds_read_b128 v[120:123], %7 offset:512\n\t" \
    "v_mfma_f32_16x16x32_bf16 %2, a[80:83], v[72:75], %2\n\t" \
    "ds_read_b128 v[124:127], %7 offset:0\n\t" \
    "v_mfma_f32_16x16x32_bf16 %1, a[48:51], v[76:79], %1\n\t" \
    "v_mfma_f32_16x16x32_bf16 %3, a[112:115], v[76:79], %3\n\t" \
    "s_waitcnt lgkmcnt(10)\n\t" \
    "v_mfma_f32_16x16x32_bf16 %2, a[20:23], v[80:83], %2\n\t" \
    "v_mfma_f32_16x16x32_bf16 %3, a[52:55], v[84:87], %3\n\t" \
    "s_waitcnt lgkmcnt(8)\n\t" \
    "v_mfma_f32_16x16x32_bf16 %0, a[20:23], v[88:91], %0\n\t" \
    "v_mfma_f32_16x16x32_bf16 %2, a[84:87], v[88:91], %2\n\t" \
    "v_mfma_f32_16x16x32_bf16 %1, a[52:55], v[92:95], %1\n\t" \
    "v_mfma_f32_16x16x32_bf16 %3, a[116:119], v[92:95], %3\n\t" \
    "s_waitcnt lgkmcnt(6)\n\t" \
    "v_mfma_f32_16x16x32_bf16 %2, a[24:27], v[96:99], %2\n\t" \
    "v_mfma_f32_16x16x32_bf16 %3, a[56:59], v[100:103], %3\n\t" \
    "s_waitcnt lgkmcnt(4)\n\t" \
    "v_mfma_f32_16x16x32_bf16 %0, a[24:27], v[104:107], %0\n\t" \
    "v_mfma_f32_16x16x32_bf16 %2, a[88:91], v[104:107], %2\n\t" \
    "v_mfma_f32_16x16x32_bf16 %1, a[56:59], v[108:111], %1\n\t" \
    "v_mfma_f32_16x16x32_bf16 %3, a[120:123], v[108:111], %3\n\t" \
    "s_waitcnt lgkmcnt(2)\n\t" \
    "v_mfma_f32_16x16x32_bf16 %2, a[28:31], v[112:115], %2\n\t" \
    "v_mfma_f32_16x16x32_bf16 %3, a[60:63], v[116:119], %3\n\t" \
    "s_waitcnt lgkmcnt(0)\n\t" \
    "v_mfma_f32_16x16x32_bf16 %0, a[28:31], v[120:123], %0\n\t" \
    "v_mfma_f32_16x16x32_bf16 %2, a[92:95], v[120:123], %2\n\t" \
    "v_mfma_f32_16x16x32_bf16 %1, a[60:63], v[124:127], %1\n\t" \
    "v_mfma_f32_16x16x32_bf16 %3, a[124:127], v[124:127], %3\n\t" \
    "s_nop 7\n\t" \
    "s_nop 1\n\t"
#define CROSS_ASM \
    "ds_read_b128 v[64:67], %5 offset:1984\n\t" \
    "ds_read_b128 v[68:71], %5 offset:1472\n\t" \
    "ds_read_b128 v[72:75], %6 offset:1984\n\t" \
    "ds_read_b128 v[76:79], %6 offset:1472\n\t" \
    "ds_read_b128 v[80:83], %5 offset:960\n\t" \
    "ds_read_b128 v[84:87], %5 offset:448\n\t" \
    "ds_read_b128 v[88:91], %6 offset:960\n\t" \
    "ds_read_b128 v[92:95], %6 offset:448\n\t" \
    "ds_read_b128 v[96:99], %5 offset:1920\n\t" \
    "ds_read_b128 v[100:103], %5 offset:1408\n\t" \
    "ds_read_b128 v[104:107], %6 offset:1920\n\t" \
    "ds_read_b128 v[108:111], %6 offset:1408\n\t" \
    "ds_read_b128 v[112:115], %5 offset:896\n\t" \
    "s_waitcnt lgkmcnt(11)\n\t" \
    "v_mfma_f32_16x16x32_bf16 %0, a[64:67], v[64:67], %0\n\t" \
    "ds_read_b128 v[116:119], %5 offset:384\n\t" \
    "v_mfma_f32_16x16x32_bf16 %1, a[96:99], v[68:71], %1\n\t" \
    "ds_read_b128 v[120:123], %6 offset:896\n\t" \
    "s_waitcnt lgkmcnt(11)\n\t" \
    "v_mfma_f32_16x16x32_bf16 %2, v[72:75], v[64:67], %2\n\t" \
    "ds_read_b128 v[124:127], %6 offset:384\n\t" \
    "v_mfma_f32_16x16x32_bf16 %3, v[76:79], v[68:71], %3\n\t" \
    "ds_read_b128 v[64:67], %5 offset:1856\n\t" \
    "s_waitcnt lgkmcnt(11)\n\t" \
    "v_mfma_f32_16x16x32_bf16 %0, v[72:75], v[80:83], %0\n\t" \
    "ds_read_b128 v[68:71], %5 offset:1344\n\t" \
    "v_mfma_f32_16x16x32_bf16 %1, v[76:79], v[84:87], %1\n\t" \
    "ds_read_b128 v[72:75], %6 offset:1856\n\t" \
    "s_waitcnt lgkmcnt(11)\n\t" \
    "v_mfma_f32_16x16x32_bf16 %2, v[88:91], v[80:83], %2\n\t" \
    "ds_read_b128 v[76:79], %6 offset:1344\n\t" \
    "v_mfma_f32_16x16x32_bf16 %3, v[92:95], v[84:87], %3\n\t" \
    "ds_read_b128 v[80:83], %5 offset:832\n\t" \
    "s_waitcnt lgkmcnt(11)\n\t" \
    "v_mfma_f32_16x16x32_bf16 %0, a[68:71], v[96:99], %0\n\t" \
    "ds_read_b128 v[84:87], %5 offset:320\n\t" \
    "v_mfma_f32_16x16x32_bf16 %1, a[100:103], v[100:103], %1\n\t" \
    "ds_read_b128 v[88:91], %6 offset:832\n\t" \
    "s_waitcnt lgkmcnt(11)\n\t" \
    "v_mfma_f32_16x16x32_bf16 %2, v[104:107], v[96:99], %2\n\t" \
    "ds_read_b128 v[92:95], %6 offset:320\n\t" \
    "v_mfma_f32_16x16x32_bf16 %3, v[108:111], v[100:103], %3\n\t" \
    "ds_read_b128 v[96:99], %5 offset:1792\n\t" \
    "s_waitcnt lgkmcnt(11)\n\t" \
    "v_mfma_f32_16x16x32_bf16 %0, v[104:107], v[112:115], %0\n\t" \
    "ds_read_b128 v[100:103], %5 offset:1280\n\t" \
    "v_mfma_f32_16x16x32_bf16 %1, v[108:111], v[116:119], %1\n\t" \
    "ds_read_b128 v[104:107], %6 offset:1792\n\t" \
    "s_waitcnt lgkmcnt(11)\n\t" \
    "v_mfma_f32_16x16x32_bf16 %2, v[120:123], v[112:115], %2\n\t" \
    "ds_read_b128 v[108:111], %6 offset:1280\n\t" \
    "v_mfma_f32_16x16x32_bf16 %3, v[124:127], v[116:119], %3\n\t" \
    "ds_read_b128 v[112:115], %5 offset:768\n\t" \
    "s_waitcnt lgkmcnt(11)\n\t" \
    "v_mfma_f32_16x16x32_bf16 %0, a[72:75], v[64:67], %0\n\t" \
    "ds_read_b128 v[116:119], %5 offset:256\n\t" \
    "v_mfma_f32_16x16x32_bf16 %1, a[104:107], v[68:71], %1\n\t" \
    "ds_read_b128 v[120:123], %6 offset:768\n\t" \
    "s_waitcnt lgkmcnt(11)\n\t" \
    "v_mfma_f32_16x16x32_bf16 %2, v[72:75], v[64:67], %2\n\t" \
    "ds_read_b128 v[124:127], %6 offset:256\n\t" \
    "v_mfma_f32_16x16x32_bf16 %3, v[76:79], v[68:71], %3\n\t" \
    "ds_read_b128 v[64:67], %5 offset:1728\n\t" \
    "s_waitcnt lgkmcnt(11)\n\t" \
    "v_mfma_f32_16x16x32_bf16 %0, v[72:75], v[80:83], %0\n\t" \
    "buffer_load_dwordx4 %4, %7, %8, %9 offen sc1\n\t" \
    "ds_read_b128 v[68:71], %5 offset:1216\n\t" \
    "v_mfma_f32_16x16x32_bf16 %1, v[76:79], v[84:87], %1\n\t" \
    "ds_read_b128 v[72:75], %6 offset:1728\n\t" \
    "s_waitcnt lgkmcnt(11)\n\t" \
    "v_mfma_f32_16x16x32_bf16 %2, v[88:91], v[80:83], %2\n\t" \
    "ds_read_b128 v[76:79], %6 offset:1216\n\t" \
    "v_mfma_f32_16x16x32_bf16 %3, v[92:95], v[84:87], %3\n\t" \
    "ds_read_b128 v[80:83], %5 offset:704\n\t" \
    "s_waitcnt lgkmcnt(11)\n\t" \
    "v_mfma_f32_16x16x32_bf16 %0, a[76:79], v[96:99], %0\n\t" \
    "ds_read_b128 v[84:87], %5 offset:192\n\t" \
    "v_mfma_f32_16x16x32_bf16 %1, a[108:111], v[100:103], %1\n\t" \
    "ds_read_b128 v[88:91], %6 offset:704\n\t" \
    "s_waitcnt lgkmcnt(11)\n\t" \
    "v_mfma_f32_16x16x32_bf16 %2, v[104:107], v[96:99], %2\n\t" \
    "ds_read_b128 v[92:95], %6 offset:192\n\t" \
    "v_mfma_f32_16x16x32_bf16 %3, v[108:111], v[100:103], %3\n\t" \
    "ds_read_b128 v[96:99], %5 offset:1664\n\t" \
    "s_waitcnt lgkmcnt(11)\n\t" \
    "v_mfma_f32_16x16x32_bf16 %0, v[104:107], v[112:115], %0\n\t" \
    "ds_read_b128 v[100:103], %5 offset:1152\n\t" \
    "v_mfma_f32_16x16x32_bf16 %1, v[108:111], v[116:119], %1\n\t" \
    "ds_read_b128 v[104:107], %6 offset:1664\n\t" \
    "s_waitcnt lgkmcnt(11)\n\t" \
    "v_mfma_f32_16x16x32_bf16 %2, v[120:123], v[112:115], %2\n\t" \
    "ds_read_b128 v[108:111], %6 offset:1152\n\t" \
    "v_mfma_f32_16x16x32_bf16 %3, v[124:127], v[116:119], %3\n\t" \
    "ds_read_b128 v[112:115], %5 offset:640\n\t" \
    "s_waitcnt lgkmcnt(11)\n\t" \
    "v_mfma_f32_16x16x32_bf16 %0, a[80:83], v[64:67], %0\n\t" \
    "ds_read_b128 v[116:119], %5 offset:128\n\t" \
    "v_mfma_f32_16x16x32_bf16 %1, a[112:115], v[68:71], %1\n\t" \
    "ds_read_b128 v[120:123], %6 offset:640\n\t" \
    "s_waitcnt lgkmcnt(11)\n\t" \
    "v_mfma_f32_16x16x32_bf16 %2, v[72:75], v[64:67], %2\n\t" \
    "ds_read_b128 v[124:127], %6 offset:128\n\t" \
    "v_mfma_f32_16x16x32_bf16 %3, v[76:79], v[68:71], %3\n\t" \
    "ds_read_b128 v[64:67], %5 offset:1600\n\t" \
    "s_waitcnt lgkmcnt(11)\n\t" \
    "v_mfma_f32_16x16x32_bf16 %0, v[72:75], v[80:83], %0\n\t" \
    "ds_read_b128 v[68:71], %5 offset:1088\n\t" \
    "v_mfma_f32_16x16x32_bf16 %1, v[76:79], v[84:87], %1\n\t" \
    "ds_read_b128 v[72:75], %6 offset:1600\n\t" \
    "s_waitcnt lgkmcnt(11)\n\t" \
    "v_mfma_f32_16x16x32_bf16 %2, v[88:91], v[80:83], %2\n\t" \
    "ds_read_b128 v[76:79], %6 offset:1088\n\t" \
    "v_mfma_f32_16x16x32_bf16 %3, v[92:95], v[84:87], %3\n\t" \
    "ds_read_b128 v[80:83], %5 offset:576\n\t" \
    "s_waitcnt lgkmcnt(11)\n\t" \
    "v_mfma_f32_16x16x32_bf16 %0, a[84:87], v[96:99], %0\n\t" \
    "ds_read_b128 v[84:87], %5 offset:64\n\t" \
    "v_mfma_f32_16x16x32_bf16 %1, a[116:119], v[100:103], %1\n\t" \
    "ds_read_b128 v[88:91], %6 offset:576\n\t" \
    "s_waitcnt lgkmcnt(11)\n\t" \
    "v_mfma_f32_16x16x32_bf16 %2, v[104:107], v[96:99], %2\n\t" \
    "ds_read_b128 v[92:95], %6 offset:64\n\t" \
    "v_mfma_f32_16x16x32_bf16 %3, v[108:111], v[100:103], %3\n\t" \
    "ds_read_b128 v[96:99], %5 offset:1536\n\t" \
    "s_waitcnt lgkmcnt(11)\n\t" \
    "v_mfma_f32_16x16x32_bf16 %0, v[104:107], v[112:115], %0\n\t" \
    "ds_read_b128 v[100:103], %5 offset:1024\n\t" \
    "v_mfma_f32_16x16x32_bf16 %1, v[108:111], v[116:119], %1\n\t" \
    "ds_read_b128 v[104:107], %6 offset:1536\n\t" \
    "s_waitcnt lgkmcnt(11)\n\t" \
    "v_mfma_f32_16x16x32_bf16 %2, v[120:123], v[112:115], %2\n\t" \
    "ds_read_b128 v[108:111], %6 offset:1024\n\t" \
    "v_mfma_f32_16x16x32_bf16 %3, v[124:127], v[116:119], %3\n\t" \
    "ds_read_b128 v[112:115], %5 offset:512\n\t" \
    "s_waitcnt lgkmcnt(11)\n\t" \
    "v_mfma_f32_16x16x32_bf16 %0, a[88:91], v[64:67], %0\n\t" \
    "ds_read_b128 v[116:119], %5 offset:0\n\t" \
    "v_mfma_f32_16x16x32_bf16 %1, a[120:123], v[68:71], %1\n\t" \
    "ds_read_b128 v[120:123], %6 offset:512\n\t" \
    "s_waitcnt lgkmcnt(11)\n\t" \
    "v_mfma_f32_16x16x32_bf16 %2, v[72:75], v[64:67], %2\n\t" \
    "ds_read_b128 v[124:127], %6 offset:0\n\t" \
    "v_mfma_f32_16x16x32_bf16 %3, v[76:79], v[68:71], %3\n\t" \
    "s_waitcnt lgkmcnt(10)\n\t" \
    "v_mfma_f32_16x16x32_bf16 %0, v[72:75], v[80:83], %0\n\t" \
    "v_mfma_f32_16x16x32_bf16 %1, v[76:79], v[84:87], %1\n\t" \
    "s_waitcnt lgkmcnt(8)\n\t" \
    "v_mfma_f32_16x16x32_bf16 %2, v[88:91], v[80:83], %2\n\t" \
    "v_mfma_f32_16x16x32_bf16 %3, v[92:95], v[84:87], %3\n\t" \
    "s_waitcnt lgkmcnt(6)\n\t" \
    "v_mfma_f32_16x16x32_bf16 %0, a[92:95], v[96:99], %0\n\t" \
    "v_mfma_f32_16x16x32_bf16 %1, a[124:127], v[100:103], %1\n\t" \
    "s_waitcnt lgkmcnt(4)\n\t" \
    "v_mfma_f32_16x16x32_bf16 %2, v[104:107], v[96:99], %2\n\t" \
    "v_mfma_f32_16x16x32_bf16 %3, v[108:111], v[100:103], %3\n\t" \
    "s_waitcnt lgkmcnt(2)\n\t" \
    "v_mfma_f32_16x16x32_bf16 %0, v[104:107], v[112:115], %0\n\t" \
    "v_mfma_f32_16x16x32_bf16 %1, v[108:111], v[116:119], %1\n\t" \
    "s_waitcnt lgkmcnt(0)\n\t" \
    "v_mfma_f32_16x16x32_bf16 %2, v[120:123], v[112:115], %2\n\t" \
    "v_mfma_f32_16x16x32_bf16 %3, v[124:127], v[116:119], %3\n\t" \
    "s_nop 7\n\t" \
    "s_nop 1\n\t" \
    "s_waitcnt vmcnt(0)\n\t"

__device__ __forceinline__ float u2f(u32 x) { return __builtin_bit_cast(float, x); }
__device__ __forceinline__ u32 f2u(float x) { return __builtin_bit_cast(u32, x); }
__device__ __forceinline__ float blo(u32 x) { return u2f(x << 16); }
__device__ __forceinline__ float bhi(u32 x) { return u2f(x & 0xffff0000u); }
__device__ __forceinline__ u32 pkbf(float a, float b) {
    v2f f = {a, b};
    return __builtin_bit_cast(u32, __builtin_convertvector(f, v2bf));
}
__device__ __forceinline__ float rbf(float a) { return blo(pkbf(a, a)); }
__device__ __forceinline__ u32 pkh(float a, float b) {
    v2f f = {a, b};
    return __builtin_bit_cast(u32, __builtin_convertvector(f, v2h));
}
template <int CTRL> __device__ __forceinline__ float dppf(float x) {
    return __builtin_bit_cast(float, __builtin_amdgcn_mov_dpp(__builtin_bit_cast(int, x), CTRL, 0xf, 0xf, true));
}
template <int CTRL> __device__ __forceinline__ u32 dppu(u32 x) {
    return (u32)__builtin_amdgcn_mov_dpp((int)x, CTRL, 0xf, 0xf, true);
}
constexpr int XOR1 = 0xB1, XOR2 = 0x4E, HMIR = 0x141, MIR = 0x140;
__device__ __forceinline__ void bar() { asm volatile("s_waitcnt lgkmcnt(0)\n\ts_barrier" ::: "memory"); }
__device__ __forceinline__ void wait_vm0() { asm volatile("s_waitcnt vmcnt(0)" ::: "memory"); }
__device__ __forceinline__ u32 umin(u32 a, u32 b) { return a < b ? a : b; }
__device__ __forceinline__ u32 umax(u32 a, u32 b) { return a > b ? a : b; }
__device__ __forceinline__ u32 ns(u32 x) { return umin(x, SENT - 1u); }
__device__ __forceinline__ bool isvalid(v4u x) { return umax(umax(x.x, x.y), umax(x.z, x.w)) != SENT; }
template <class T> __device__ __forceinline__ void pin(T& x) { asm volatile("" : "+v"(x)); }

typedef __amdgpu_buffer_rsrc_t rsrc_t;
__device__ __forceinline__ rsrc_t mkr(const void* p, u32 bytes) { return __builtin_amdgcn_make_buffer_rsrc((void*)p, 0, bytes, 0x00020000); }
__device__ __forceinline__ void st16(rsrc_t r, u32 off, u32 so, v4u v) {
    asm volatile("" : "+v"(v));
    __builtin_amdgcn_raw_buffer_store_b128(v, r, off, so, 16);
    asm volatile("s_nop 1" :: "v"(v));
}
__device__ __forceinline__ void st8(rsrc_t r, u32 off, u32 so, v2u v) { __builtin_amdgcn_raw_buffer_store_b64(v, r, off, so, 16); }
__device__ __forceinline__ v4u ld16(rsrc_t r, u32 off, u32 so) { return __builtin_amdgcn_raw_buffer_load_b128(r, off, so, 16); }
__device__ __forceinline__ u32 ldw(rsrc_t r, u32 off, u32 so) { return __builtin_amdgcn_raw_buffer_load_b32(r, off, so, 0); }
__device__ __forceinline__ u32 ldh(rsrc_t r, u32 off, u32 so) { return (u32)__builtin_amdgcn_raw_buffer_load_b16(r, off, so, 0); }

__device__ __forceinline__ void wait_flag(rsrc_t r, u32 off, v2u tag, bool& dead) {
    u32 spins = 0;
    while (!dead) {
        asm volatile("" ::: "memory");
        const v2u x = __builtin_amdgcn_raw_buffer_load_b64(r, off, 0, 16);
        if (x.x == tag.x && x.y == tag.y) break;
        if (++spins > SPIN_CAP) dead = true;
    }
}
__device__ __forceinline__ void repoll16(rsrc_t r, u32 off, u32 so, v4u& x, bool& dead) {
    u32 spins = 0;
    while (!isvalid(x) && !dead) {
        asm volatile("" ::: "memory");
        x = ld16(r, off, so);
        if (++spins > SPIN_CAP) dead = true;
    }
}

__device__ __forceinline__ void build_T(char* base, v4u lo, v4u hi) {
    const u32 W[8] = {lo.x, lo.y, lo.z, lo.w, hi.x, hi.y, hi.z, hi.w};
    u32 R[8], B[8];
#pragma unroll
    for (int m = 1; m < 8; ++m) {
        R[m] = __builtin_amdgcn_alignbit(W[m], W[m], 16);
        B[m] = (W[m] & 0xffffu) | (W[m - 1] & 0xffff0000u);
    }
#pragma unroll
    for (int rho = 0; rho < 8; ++rho) {
        const int idx = rho < 4 ? 2 * rho : 2 * (rho - 4) + 1;
        v4u o;
        if (rho & 1) { const int m = (7 + rho) / 2; o = (v4u){R[m], R[m - 1], R[m - 2], R[m - 3]}; }
        else { const int m = 4 + rho / 2; o = (v4u){B[m], B[m - 1], B[m - 2], B[m - 3]}; }
        *(v4u*)(base + idx * 258 * 16) = o;
    }
}

__device__ __forceinline__ void to_rowlay(v4u a, u32 (&o)[4]) {
    auto s0 = __builtin_amdgcn_permlane32_swap(a.x, a.z, false, false);
    auto s1 = __builtin_amdgcn_permlane32_swap(a.y, a.w, false, false);
    auto s2 = __builtin_amdgcn_permlane16_swap(s0[0], s1[0], false, false);
    auto s3 = __builtin_amdgcn_permlane16_swap(s0[1], s1[1], false, false);
    o[0] = s2[0]; o[1] = s2[1]; o[2] = s3[0]; o[3] = s3[1];
}
__device__ __forceinline__ v4u from_rowlay(const u32 (&o)[4]) {
    auto s2 = __builtin_amdgcn_permlane16_swap(o[0], o[1], false, false);
    auto s3 = __builtin_amdgcn_permlane16_swap(o[2], o[3], false, false);
    auto s0 = __builtin_amdgcn_permlane32_swap(s2[0], s3[0], false, false);
    auto s1 = __builtin_amdgcn_permlane32_swap(s2[1], s3[1], false, false);
    return (v4u){s0[0], s1[0], s0[1], s1[1]};
}

__device__ __forceinline__ void lsig(u32 addr) {
    asm volatile("s_mov_b64 exec, 1\n\tds_add_u32 %0, %1\n\ts_mov_b64 exec, -1" :: "v"(addr), "v"(LINC) : "memory");
}
#define SETPRIO(x) __builtin_amdgcn_s_setprio(x)
__device__ __forceinline__ void lwait(const char* p, u32 target) {
    u32 tg = target * LINC + 1u;
    do {
        asm volatile("" ::: "memory");
        --tg;
    } while ((u32)__builtin_amdgcn_readfirstlane(*(const u32*)p) < tg);
    asm volatile("" ::: "memory");
}
__device__ __forceinline__ bool lpeek(const char* p, u32 target) {
    asm volatile("" ::: "memory");
    return (u32)__builtin_amdgcn_readfirstlane(*(const u32*)p) >= target * LINC;
}
#define POLL_HEAD "s_mov_b64 vcc, -1\n\t1:\n\ts_mov_b64 exec, vcc\n\t"
#define POLL_MID "s_mov_b64 exec, -1\n\ts_sub_i32 %0, %0, 1\n\ts_cmp_gt_i32 %0, 0\n\ts_cselect_b64 %1, -1, 0\n\t"
#define POLL_TAIL "s_and_b64 vcc, vcc, %1\n\ts_cbranch_scc1 1b\n\t"
__device__ __forceinline__ void poll2(rsrc_t ws, u32 off, u32 so0, u32 so1, v4u& a, v4u& b, int& bud) {
    u64 m; u32 t;
    asm volatile(POLL_HEAD
        "buffer_load_dwordx4 v[56:59], %5, %6, %7 offen sc1\n\t"
        "buffer_load_dwordx4 v[60:63], %5, %6, %8 offen sc1\n\t"
        POLL_MID
        "s_waitcnt vmcnt(1)\n\t"
        "v_max3_u32 %2, v56, v57, v58\n\t"
        "s_waitcnt vmcnt(0)\n\t"
        "v_max3_u32 %2, v59, v60, %2\n\t"
        "v_max3_u32 %2, v61, v62, %2\n\t"
        "v_max_u32 %2, v63, %2\n\t"
        "v_cmp_eq_u32 vcc, -1, %2\n\t"
        POLL_TAIL
        : "+s"(bud), "=&s"(m), "=&v"(t), "=&{v[56:59]}"(a), "=&{v[60:63]}"(b) : "v"(off), "s"(ws), "s"(so0), "s"(so1) : "memory", "vcc", "scc");
}
__device__ __forceinline__ void poll1(rsrc_t ws, u32 off, u32 so, v4u& a, int& bud) {
    u64 m; u32 t;
    asm volatile(POLL_HEAD
        "buffer_load_dwordx4 v[56:59], %4, %5, %6 offen sc1\n\t"
        POLL_MID
        "s_waitcnt vmcnt(0)\n\t"
        "v_max3_u32 %2, v56, v57, v58\n\t"
        "v_max_u32 %2, v59, %2\n\t"
        "v_cmp_eq_u32 vcc, -1, %2\n\t"
        POLL_TAIL
        : "+s"(bud), "=&s"(m), "=&v"(t), "=&{v[56:59]}"(a) : "v"(off), "s"(ws), "s"(so) : "memory", "vcc", "scc");
}
__device__ __forceinline__ void pollf(rsrc_t ws, u32 off, u32 so, u64 f, u64 tag, int& bud) {
    if (__builtin_amdgcn_ballot_w64(f != tag) != 0) {
        u64 m;
        asm volatile(POLL_HEAD
            "buffer_load_dwordx2 %2, %3, %4, %5 offen sc1\n\t"
            POLL_MID
            "s_waitcnt vmcnt(0)\n\t"
            "v_cmp_ne_u64 vcc, %6, %2\n\t"
            POLL_TAIL
            : "+s"(bud), "=&s"(m), "=&v"(f) : "v"(off), "s"(ws), "s"(so), "s"(tag) : "memory", "vcc", "scc");
    }
}
__device__ __forceinline__ u64 ldf(rsrc_t ws, u32 off, u32 so) { return __builtin_bit_cast(u64, __builtin_amdgcn_raw_buffer_load_b64(ws, off, so, 16)); }

struct Stat { float ma, mb, ra, rb; };
__device__ __forceinline__ void row_stats(char* rr, int r, int lane, const float (&da)[8], const float (&db)[8], Stat& s, float& na, float& nb) {
    float v0 = ((da[0] + da[1]) + (da[2] + da[3])) + ((da[4] + da[5]) + (da[6] + da[7]));
    float v2 = ((db[0] + db[1]) + (db[2] + db[3])) + ((db[4] + db[5]) + (db[6] + db[7]));
    float v1 = da[0] * da[0], v1b = da[1] * da[1], v3 = db[0] * db[0], v3b = db[1] * db[1];
#pragma unroll
    for (int k = 2; k < 8; k += 2) {
        v1 = __builtin_fmaf(da[k], da[k], v1); v1b = __builtin_fmaf(da[k + 1], da[k + 1], v1b);
        v3 = __builtin_fmaf(db[k], db[k], v3); v3b = __builtin_fmaf(db[k + 1], db[k + 1], v3b);
    }
    v1 += v1b; v3 += v3b;
    asm volatile(
        "s_nop 1\n\t"
        "v_add_f32_dpp %0, %0, %0 row_ror:8 row_mask:0xf bank_mask:0x3\n\t"
        "v_add_f32_dpp %1, %1, %1 row_ror:8 row_mask:0xf bank_mask:0x3\n\t"
        "v_add_f32_dpp %0, %2, %2 row_ror:8 row_mask:0xf bank_mask:0xc\n\t"
        "v_add_f32_dpp %1, %3, %3 row_ror:8 row_mask:0xf bank_mask:0xc\n\t"
        "s_nop 0\n\t"
        "v_add_f32_dpp %0, %0, %0 row_half_mirror row_mask:0xf bank_mask:0x5\n\t"
        "v_add_f32_dpp %0, %1, %1 row_half_mirror row_mask:0xf bank_mask:0xa\n\t"
        "s_nop 1\n\t"
        "v_add_f32_dpp %0, %0, %0 quad_perm:[1,0,3,2] row_mask:0xf bank_mask:0xf\n\t"
        "s_nop 1\n\t"
        "v_add_f32_dpp %0, %0, %0 quad_perm:[2,3,0,1] row_mask:0xf bank_mask:0xf\n\t"
        : "+v"(v0), "+v"(v1) : "v"(v2), "v"(v3));
    *(u32*)(rr + (lane >> 4) * 64 + r * 16 + (lane & 12)) = ns(f2u(v0));
    v4u zi;
    u32 spins = 0;
    do {
        asm volatile("" ::: "memory");
        zi = *(const v4u*)(rr + (lane >> 4) * 64 + (lane & 3) * 16);
    } while (__builtin_amdgcn_ballot_w64(!isvalid(zi)) != 0 && ++spins < LSPIN_CAP);
    v4f z = __builtin_bit_cast(v4f, zi);
#pragma unroll
    for (int k = 0; k < 4; ++k) z[k] += dppf<XOR1>(z[k]);
#pragma unroll
    for (int k = 0; k < 4; ++k) z[k] += dppf<XOR2>(z[k]);
    const float inv = 1.f / DM;
    const float mua = z[0] * inv, mub = z[2] * inv;
    const float va = __builtin_fmaxf(__builtin_fmaf(-mua, mua, z[1] * inv), 0.f), vb = __builtin_fmaxf(__builtin_fmaf(-mub, mub, z[3] * inv), 0.f);
    const u32 pv = pkbf(va, vb);
    const u32 pe = pkbf(blo(pv) + 1e-5f, bhi(pv) + 1e-5f);
    const u32 pd = pkbf(__builtin_amdgcn_sqrtf(blo(pe)), __builtin_amdgcn_sqrtf(bhi(pe)));
    const u32 pm = pkbf(s.ma + mua, s.mb + mub);
    s.ra = __builtin_amdgcn_rcpf(blo(pd));
    s.rb = __builtin_amdgcn_rcpf(bhi(pd));
    na = (s.ma - blo(pm)) * s.ra;
    nb = (s.mb - bhi(pm)) * s.rb;
    s.ma = blo(pm);
    s.mb = bhi(pm);
}
#define MFMA(a, b, c) __builtin_amdgcn_mfma_f32_16x16x32_bf16(a, b, c, 0, 0, 0)
#define SGB(m, n) __builtin_amdgcn_sched_group_barrier(m, n, 0)
__device__ __forceinline__ u32 ln2(u32 x, const Stat& s) {
    const u32 pd = pkbf(blo(x) - s.ma, bhi(x) - s.mb);
    return pkbf(blo(pd) * s.ra, bhi(pd) * s.rb);
}
__device__ __forceinline__ u32 add2(u32 a, u32 b) { return pkbf(blo(a) + blo(b), bhi(a) + bhi(b)); }
__device__ __forceinline__ u32 mul2(u32 a, u32 b) { return pkbf(blo(a) * blo(b), bhi(a) * bhi(b)); }
__device__ __forceinline__ v4u pair_xpose(bool odd, u32 c0a, u32 c0b, u32 c1a, u32 c1b) {
    const u32 sa = odd ? c0a : c1a, sb = odd ? c0b : c1b;
    const u32 ra = dppu<XOR1>(sa), rb = dppu<XOR1>(sb);
    return (v4u){odd ? ra : c0a, odd ? rb : c0b, odd ? c1a : ra, odd ? c1b : rb};
}

struct cf { float r, i; };
__device__ __forceinline__ cf cmul(cf a, cf b) { return {__builtin_fmaf(a.r, b.r, -(a.i * b.i)), __builtin_fmaf(a.r, b.i, a.i * b.r)}; }
template <int K> __device__ __forceinline__ cf cexpk(float la, float th, float tl) {
    const float sc = (float)(1 << K);
    const float mag = __builtin_amdgcn_exp2f(la * sc);
    const float ph = __builtin_fmaf(tl, sc, __builtin_amdgcn_fractf(th * sc));
    return {mag * __builtin_amdgcn_cosf(ph), mag * __builtin_amdgcn_sinf(ph)};
}
__device__ __forceinline__ float exp_acc(float x) {
    const float y = x * 1.44269504088896340736f;
    const float n = __builtin_rintf(y);
    float f = __builtin_fmaf(x, 1.44269504088896340736f, -n);
    f = __builtin_fmaf(x, 1.92596299e-8f, f);
    const float nc = __builtin_fminf(__builtin_fmaxf(n, -300.f), 300.f);
    const float e = __builtin_ldexpf(__builtin_amdgcn_exp2f(f), (int)nc);
    return __builtin_fabsf(y) < 400.f ? e : __builtin_amdgcn_exp2f(y);
}

#define DL0 0
#define DL1 DL0
#define OUT_AUX 16
__device__ __forceinline__ void st16o(rsrc_t r, u32 off, u32 so, v4u v) {
    asm volatile("" : "+v"(v));
    __builtin_amdgcn_raw_buffer_store_b128(v, r, off, so, OUT_AUX);
    asm volatile("s_nop 1" :: "v"(v));
}
#define DLY_U0 210
#define DLY_U1 260
#define DLY_Y 14
#define DLY_K 6
#define DLY_S 4
#define SSMK_POS 0
#define Q_POS 0
#define YC_POS 1
#define CPRIO 1
#define RPRIO 0
#define XPRIO 0
#define ORDER 1
#define PRE_ADV 95
#define NOROW 0
#define YL 0
#define CPF 4
#define CODE_BYTES 23936
#define HB 1
struct KG { float la, th, tl, dt; };
struct KP { u32 a, im, dt; };
__device__ __forceinline__ KP kg_load(const Params& p, int chan, int kn) {
    KP k;
    const u32 so = (u32)chan * (NST * 2);
    k.a = ldh(mkr(p.logA, DM * NST * 2), kn * 2, so);
    k.dt = ldh(mkr(p.logdt, DM * 2), 0, chan * 2);
    k.im = ldh(mkr(p.Aim, DM * NST * 2), kn * 2, so);
    return k;
}
__device__ __forceinline__ KG kg_prep(const KP& k) {
    KG g;
    g.dt = rbf(exp_acc(blo(k.dt)));
    const float zr = -rbf(exp_acc(blo(k.a))) * g.dt;
    const float zi = blo(k.im) * g.dt;
    g.la = zr * 1.44269504088896340736f;
    g.th = zi * 0.15915494309189533577f;
    g.tl = __builtin_fmaf(zi, 6.4206383e-9f, __builtin_fmaf(zi, 0.15915494309189533577f, -g.th));
    return g;
}
#define FENCE() asm volatile("" ::: "memory")
#define PUTH(row, h) do { const cf h_ = (h); *(u32*)(scr + (row) * SCR_ROW) = pkh(h_.r, -h_.i); } while (0)
#define PUTL(row, l) do { const cf l_ = (l); *(u32*)(scr + (41 + (row)) * SCR_ROW) = pkh(l_.r, l_.i); } while (0)
#define CEXP(k) cexpk<k>(g.la, g.th, g.tl)
#define PUTP(row, v) (*(u32*)(scr + (row) * SCR_ROW) = (v))
__device__ __forceinline__ u32 pkc(cf a) { return pkh(a.r, -a.i); }
__device__ __forceinline__ u32 pkp(cf a) { return pkh(a.r, a.i); }
__device__ __forceinline__ u32 hmul(u32 x, u32 y) {
    u32 t;
    asm("v_pk_mul_f16 %0, %1, %2 op_sel:[0,0] op_sel_hi:[0,1]\n\t"
        "v_pk_fma_f16 %0, %1, %2, %0 op_sel:[1,1,0] op_sel_hi:[1,0,1] neg_lo:[0,1,0]" : "=&v"(t) : "v"(x), "v"(y));
    return t;
}
#define DSK(k) (((k) & 2) ? bhi(Dsk[((k) >> 2) * 2 + ((k) & 1)]) : blo(Dsk[((k) >> 2) * 2 + ((k) & 1)]))
__device__ __forceinline__ v4u half_xpose(u32 c0a, u32 c0b, u32 c1a, u32 c1b) {
    auto sa = __builtin_amdgcn_permlane32_swap(c0a, c1a, false, false);
    auto sb = __builtin_amdgcn_permlane32_swap(c0b, c1b, false, false);
    return (v4u){sa[0], sb[0], sa[1], sb[1]};
}

__device__ __forceinline__ void get_y(rsrc_t ws, u32 off, u32 so0, u32 so1, u32 (&y)[8], int& dead) {
    v4u y0, y1;
    poll2(ws, off, so0, so1, y0, y1, dead);
    u32 o[4];
    to_rowlay(y0, o);
#pragma unroll
    for (int k = 0; k < 4; ++k) y[k] = o[k];
    to_rowlay(y1, o);
#pragma unroll
    for (int k = 0; k < 4; ++k) y[4 + k] = o[k];
}
__device__ __forceinline__ void put_u(rsrc_t ws, u32 off, u32 so, const float (&da)[8], const float (&db)[8], const Stat& s, float na, float nb) {
#pragma unroll
    for (int e = 0; e < 2; ++e) {
        u32 uh[4];
#pragma unroll
        for (int k = 0; k < 4; ++k) uh[k] = ns(pkbf(__builtin_fmaf(da[e * 4 + k], s.ra, na), __builtin_fmaf(db[e * 4 + k], s.rb, nb)));
        st16(ws, off, so + e * 262144u, from_rowlay(uh));
    }
}

__global__ void __launch_bounds__(512, 2) mega(Params p) {
    __shared__ __attribute__((aligned(16))) char smem[L_TOTAL];
    const int t = __builtin_amdgcn_workitem_id_x(), lane = t & 63;
    u32 pf;
    {
        u64 pc0;
        const u32 po = umin((u32)(t >> 6) * 3072u + umin(lane, 23) * 128u, CODE_BYTES);
        asm volatile("s_getpc_b64 %0\n\tglobal_load_dword %1, %2, %0" : "=&s"(pc0), "=&v"(pf) : "v"(po));
    }
    const int w = __builtin_amdgcn_readfirstlane(t >> 6);
    const int b = __builtin_amdgcn_workgroup_id_x();
    const int cid = (b & 7) * 32 + (b >> 3);
    const int half = (b >> (3 + HB)) & 1;
    const int rid = half * 128 + (b & 7) * 16 + ((b >> (4 + HB)) << HB) + ((b >> 3) & ((1 << HB) - 1));
    const rsrc_t ws = mkr(p.ws, WS_BYTES);
    int dead = SPIN_CAP;
    const v4u sent4 = {SENT, SENT, SENT, SENT};
#define CLR(o, so, lvl) st16(ws, o, so, sent4)
    const int li = lane & 15, lg = lane >> 4;
    const u32 sbase = (u32)(size_t)(LAS char*)smem;
    STAMP(0);
    asm volatile("" :: "s"(p.q), "s"(p.k), "s"(p.v), "s"(p.sk), "s"(p.ssmk), "s"(p.ssmD), "s"(p.logA), "s"(p.Aim), "s"(p.Cr), "s"(p.Ci), "s"(p.logdt), "s"(p.Dskip), "s"(p.out));
    u64 ep = *(const __attribute__((address_space(4))) u64*)(p.ws + WS_EPOCH);
#define BAR_EP() asm volatile("s_waitcnt lgkmcnt(0)\n\ts_barrier" : "+s"(ep) :: "memory")
    const int kch = (w >> 1) & 1, kn = (w & 1) * 64 + lane;
    char* scr = smem + L_SCR + kch * SCR_CH + kn * 4;
    const int slot = cid * 2 + kch;
    const int chan = (slot & ~63) + 2 * (slot & 31) + ((slot >> 5) & 1);
    const u32 o_w = (u32)lg * 65536u + (u32)li * 16u;
    const u32 s_w = WS_U + (u32)rid * 256u + (u32)(w & 3) * 524288u;
    const u32 o_yc = (u32)((lane >> 3) & 3) * 65536u + (u32)(lane >> 5) * 128u + (u32)(lane & 7) * 16u;
    const u32 s_yc = WS_Y + (u32)w * 262144u + (u32)cid * 256u;
    const u32 s_yw = WS_Y + (u32)(w & 1) * 131072u + (u32)slot * 128u;
#define TAG() u64 e1_ = ep; asm volatile("" : "+s"(e1_)); e1_ += 1ull; const v2u tag = {(u32)e1_, (u32)(e1_ >> 32)}
    const int r = w & 3;
    const u32 o_rw = (u32)(lane >> 5) * 4096u + (u32)(lane & 31) * 4u;
    const u32 s_rw = (u32)rid * 8192u + (u32)r * 256u;
    u32 kk[2][7], vv[2][4], Dch[2];
    v4u sk4[2];
    {
        const u32 oc = t < 256 ? o_w : 0x80000000u;
        CLR(oc, s_w, 3);
        CLR(oc, s_w + 262144u, 3);
    }
    KP kp;
    u32 pcr, pci, pcr2, pci2;
    {
        const u32 nb = w < 4 ? DM * NST * 2 : 0;
        asm volatile("buffer_load_ushort %0, %7, %9, %14 offen\n\tbuffer_load_ushort %1, off, %10, %15\n\tbuffer_load_ushort %2, %7, %11, %14 offen\n\t"
                     "buffer_load_ushort %3, %7, %12, %14 offen\n\tbuffer_load_ushort %4, %7, %13, %14 offen\n\t"
                     "buffer_load_ushort %5, %8, %12, %14 offen\n\tbuffer_load_ushort %6, %8, %13, %14 offen"
                     : "=&v"(kp.a), "=&v"(kp.dt), "=&v"(kp.im), "=&v"(pcr), "=&v"(pci), "=&v"(pcr2), "=&v"(pci2)
                     : "v"(kn * 2), "v"((kn ^ 64) * 2), "s"(mkr(p.logA, DM * NST * 2)), "s"(mkr(p.logdt, DM * 2)), "s"(mkr(p.Aim, DM * NST * 2)),
                       "s"(mkr(p.Cr, nb)), "s"(mkr(p.Ci, nb)), "s"(chan * (NST * 2)), "s"(chan * 2) : "memory");
    }
    FENCE();
    if (w >= 4) {
    {
        const rsrc_t rk = mkr(p.k, SL * DM * 2), rv = mkr(p.v, SL * DM * 2);
        const u32 ok = o_rw + s_rw - 3072u;
#pragma unroll
        for (int e = 0; e < 2; ++e) {
#pragma unroll
            for (int j = 0; j < 3; ++j) kk[e][j] = ldw(rk, ok + j * 1024u + e * 128u, 0);
#pragma unroll
            for (int j = 0; j < 4; ++j) kk[e][3 + j] = ldw(rk, o_rw + j * 1024u + e * 128u, s_rw);
#pragma unroll
            for (int j = 0; j < 4; ++j) vv[e][j] = ldw(rv, o_rw + j * 1024u + e * 128u, s_rw);
            sk4[e] = __builtin_amdgcn_raw_buffer_load_b128(mkr(p.sk, DM * 8), (lane & 31) * 16 + e * 512, r * 1024, 0);
            Dch[e] = ldh(mkr(p.ssmD, DM * 2), (lane & 31) * 4 + (lane >> 5) * 2 + e * 128, r * 256);
        }
    }
    }
    {
        u32 ia = L_U + (t >> 6) * U_CH + lane * 16;
        ia = t >= 256 ? L_RR + lane * 16 : ia;
        ia = t >= 320 ? L_CNT + li * 16 : ia;
        ia = t >= 384 ? L_T + (li >> 3) * T_CH + ((li & 7) * 258 + 257) * 16 : ia;
        ia = t >= 448 ? L_KL + (lane & 1) * KL_CH : ia;
        const u32 iv = (t >> 6) == 4 ? SENT : 0u;
        *(v4u*)(smem + ia) = (v4u){iv, iv, iv, iv};
    }
    STAMP(6);
    asm volatile("s_waitcnt lgkmcnt(0)\n\ts_barrier\n\ts_waitcnt vmcnt(26)\n\ts_cmp_lt_u32 %9, 4\n\ts_cbranch_scc0 1f\n\ts_waitcnt vmcnt(0)\n\t1:"
                 : "+s"(ep), "+v"(kp.a), "+v"(kp.dt), "+v"(kp.im), "+v"(pcr), "+v"(pci), "+v"(pcr2), "+v"(pci2), "+v"(pf) : "s"(w) : "memory", "scc");
    if (w < 4) {
        const int ch = w >> 1, par = w & 1;
        v4u sa0[2], sa1[2];
        auto late_loads = [&]() {
            FENCE();
        {
            const rsrc_t rs = mkr(p.ssmk + (size_t)chan * SL, SL * 2);
            const u32 o = (u32)(255 - (t & 127)) * 16u;
            sa1[0] = __builtin_amdgcn_raw_buffer_load_b128(rs, o, 0, 0);
            sa0[0] = __builtin_amdgcn_raw_buffer_load_b128(rs, o - 16u, 0, 0);
            sa1[1] = __builtin_amdgcn_raw_buffer_load_b128(rs, o - 2048u, 0, 0);
            sa0[1] = __builtin_amdgcn_raw_buffer_load_b128(rs, o - 2064u, 0, 0);
        }
        FENCE();
        };
        TAG();
        const u32 o_fs = lane == 0 ? WS_FLAG : 0x80000000u;
#define STFLAG(so) st8(ws, o_fs, so, tag)
        lsig(sbase + C_K1);
        STFLAG((w * 256 + cid) * 8);
        STAMP(7);
        const u32 pf1 = ldw(ws, (u32)lane * MB2 + (WS_U - 128u), 0);
        const u32 pf2 = ldw(mkr(p.out, SL * DM * 2), 0, rid * 8192);
        auto y0_clear = [&]() {
            FENCE();
            CLR(o_yc, s_yc, 2);
            CLR(o_yc, s_yc + MB1, 2);
            FENCE();
        };
        if (ORDER != 3) y0_clear();
        if (ORDER != 2 && ORDER != 4) late_loads();
        SETPRIO(CPRIO);
        {
            const KG g = kg_prep(kp);
            u32 cm = umax(umax(pcr & 0x7fffu, pci & 0x7fffu), umax(pcr2 & 0x7fffu, pci2 & 0x7fffu));
            cm = umax(cm, dppu<XOR1>(cm));
            cm = umax(cm, dppu<XOR2>(cm));
            cm = umax(cm, dppu<HMIR>(cm));
            cm = umax(cm, dppu<MIR>(cm));
            { auto sw = __builtin_amdgcn_permlane16_swap(cm, cm, false, false); cm = umax(sw[0], sw[1]); }
            { auto sw = __builtin_amdgcn_permlane32_swap(cm, cm, false, false); cm = umax(sw[0], sw[1]); }
            const int e = (int)(cm >> 7);
            const int sf = 267 - e < 253 ? 267 - e : 253;
            const float scale = u2f((u32)sf << 23);
            *(float*)(smem + L_YSC + kch * 4) = 2.f * g.dt * u2f((u32)(254 - sf) << 23);
            cf G[4], F[8];
            G[0] = {blo(pcr) * scale, blo(pci) * scale};
            const cf E7 = CEXP(7), E8 = CEXP(8), E9 = CEXP(9);
            G[1] = cmul(G[0], E7); G[2] = cmul(G[0], E8); G[3] = cmul(G[1], E8);
            F[1] = CEXP(0); F[2] = CEXP(1); F[4] = CEXP(2);
            F[3] = cmul(F[1], F[2]); F[5] = cmul(F[1], F[4]); F[6] = cmul(F[2], F[4]); F[7] = cmul(F[3], F[4]);
            u32 gp[4], fp[8];
#pragma unroll
            for (int a = 0; a < 4; ++a) gp[a] = pkc(G[a]);
#pragma unroll
            for (int a = 1; a < 8; ++a) fp[a] = pkc(F[a]);
            PUTP(0, gp[0]); PUTP(8, gp[1]); PUTP(16, gp[2]); PUTP(20, gp[3]);
            if (ORDER == 3) y0_clear();
            if (ORDER == 2) late_loads();
#pragma unroll
            for (int e2 = 1; e2 < 8; ++e2) { PUTP(e2, hmul(gp[0], fp[e2])); PUTP(8 + e2, hmul(gp[1], fp[e2])); }
#pragma unroll
            for (int e2 = 1; e2 < 4; ++e2) { PUTP(16 + e2, hmul(gp[2], fp[2 * e2])); PUTP(20 + e2, hmul(gp[3], fp[2 * e2])); }
            const u32 e9 = pkc(E9), e10 = pkc(CEXP(10));
#pragma unroll
            for (int a = 0; a < 4; ++a) {
                const u32 h = pkc(cmul(G[a], E9));
                PUTP(24 + 2 * a, h); PUTP(25 + 2 * a, hmul(h, fp[4])); PUTP(32 + a, hmul(h, e9)); PUTP(36 + a, hmul(h, e10));
            }
            PUTP(40, hmul(gp[0], pkc(CEXP(11))));
        }
        lsig(sbase + C_K);
        STAMP(1);
        const u32 o_fl1 = WS_FLAG + (u32)((cid >> 6) * 256 + (t >> 5) * 32 + (t & 1) * 16 + ((t >> 1) & 15)) * 8u;
        asm volatile("s_waitcnt vmcnt(4)" ::: "memory");
        if (ORDER != 3) STFLAG((w * 256 + cid) * 8 + 8192);
        asm volatile("" :: "v"(pf1), "v"(pf2));
        char* tslot = smem + L_T + ch * T_CH + ((t & 127) + 1) * 16;
        build_T(tslot, sa0[0], sa1[0]);
        FENCE();
        u64 fl = ldf(ws, o_fl1, 0);
        FENCE();
        build_T(tslot + 2048, sa0[1], sa1[1]);
        lsig(sbase + C_CONV);
        const int rho = li & 7;
        const u32 tb = sbase + L_T + ch * T_CH + ((rho < 4 ? 2 * rho : 2 * (rho - 4) + 1) * 258 + 2 - (li >> 3) + lg) * 16;
        const u32 ub = sbase + L_U + ch * U_CH + (4 * (li & 8) + 16 * par + 2 * (li & 7) + lg + 2) * 16;
        const u32 o_yw = (u32)(lg & 1) * 524288u + (u32)(li & 8) * 32768u + (u32)(li & 4) * 16384u + (u32)(li & 3) * 32u + (u32)(lg & 2) * 8u;
        const u32 o_ur = (u32)(t >> 1) * 256u + (u32)(t & 1) * 16u;
        const u32 s_ur = WS_U + (u32)(cid >> 3) * 65536u + (u32)(cid & 7) * 32u;
        char* uslot = smem + L_U + (t & 1) * U_CH + (64 + (t >> 1)) * 16;
        u32 tq = (u32)__builtin_amdgcn_s_memrealtime();
        STAMP(26);
        lwait(smem + C_CONV, 4);
        STAMP(2);
        asm volatile(TLOAD_ASM :: "v"(tb) : "memory", TLOAD_CLOB);
        pollf(ws, o_fl1, 0, fl, e1_, dead);
        if (NOROW) __builtin_amdgcn_s_sleep(NOROW);
        v4u uin;
        u32 tg = 8 * LINC + 1u;
#define PUT_Y(so) do { \
            a0 += a1; a2 += a3; \
            auto s0 = __builtin_amdgcn_permlane16_swap(ns(pkbf(a0[0], a0[1])), ns(pkbf(a2[0], a2[1])), false, false); \
            auto s1 = __builtin_amdgcn_permlane16_swap(ns(pkbf(a0[2], a0[3])), ns(pkbf(a2[2], a2[3])), false, false); \
            st16(ws, o_yw, so, (v4u){s0[0], s1[0], s0[1], s1[1]}); \
            lsig(sbase + C_Y); } while (0)
#define TRI(uu) do { u32 t1_ = tg, t2_; \
            asm volatile(TRI_ASM : "+v"(a0), "+v"(a1), "+v"(a2), "+v"(a3), "+s"(t1_), "=&s"(t2_) : "v"(sbase + C_CONV), "v"(uu) : "memory", "scc", CONV_CLOB); \
            tg += 4 * LINC; } while (0)
#pragma unroll 1
        for (int L = 0;; ++L) {
            const int uo = (L & 1) * 2 * U_CH;
            const u32 lo = (u32)L * MB2;
            const u32 u = ub + uo;
            if (L) STAMP(3 + L * 8);
            poll1(ws, o_ur, s_ur + lo, uin, dead);
            *(v4u*)(uslot + uo) = uin;
            lsig(sbase + C_CONV);
            u64 tn = __builtin_amdgcn_s_memrealtime();
            const u32 oob = L == 2 ? 0x80000000u : 0u;
            CLR(o_w | oob, s_w + lo + MB2, 1);
            CLR(o_w | oob, s_w + lo + MB2 + 262144u, 1);
            v4f a0 = {0.f, 0.f, 0.f, 0.f}, a1 = a0, a2 = a0, a3 = a0;
            STAMP(4 + L * 8);
            TRI(u);
            PUT_Y(s_yw + lo);
            STAMP(5 + L * 8);
            CLR(o_yc | oob, s_yc + lo + MB2, 1);
            CLR(o_yc | oob, s_yc + lo + MB2 + MB1, 1);
            a0 = (v4f){0.f, 0.f, 0.f, 0.f}; a1 = a0; a2 = a0; a3 = a0;
            asm volatile("" : "+s"(tn));
            const bool pre = (int)((u32)tn + PRE_ADV - tq) >= 0;
            asm volatile(CROSS_ASM : "+v"(a0), "+v"(a1), "+v"(a2), "+v"(a3), "=&v"(uin)
                         : "v"(u), "v"(tb), "v"(pre ? o_ur : 0x80000000u), "s"(ws), "s"(s_ur + lo + 32768u) : "memory", CONV_CLOB);
            lsig(sbase + C_CL);
            STAMP(6 + L * 8);
            if (!pre || __builtin_amdgcn_ballot_w64(!isvalid(uin)) != 0) {
                u32 n = 0;
#pragma nounroll
                while ((int)((u32)__builtin_amdgcn_s_memrealtime() - tq) < 0 && ++n < 1000) __builtin_amdgcn_s_sleep(1);
                poll1(ws, o_ur, s_ur + lo + 32768u, uin, dead);
            }
            *(v4u*)(uslot + uo + 2048) = uin;
            lsig(sbase + C_CONV);
            STFLAG((w * 256 + cid) * 8 + 16384 + L * 8192);
            STAMP(8 + L * 8);
            TRI(u + 2048);
            PUT_Y(s_yw + lo + MB1);
            STAMP(9 + L * 8);
            if (L == 2) break;
            tq = (u32)__builtin_amdgcn_s_memrealtime() + (L == 0 ? DLY_U0 : DLY_U1);
            if (L == 0) {
                lwait(smem + C_ROW, 12);
                STAMP(10);
                const char* kl = smem + L_KL + ch * KL_CH + (127 - (t & 127)) * 16;
                build_T(tslot, *(const v4u*)(kl + 2048), *(const v4u*)(kl + 2064));
                build_T(tslot + 2048, *(const v4u*)kl, *(const v4u*)(kl + 16));
                lsig(sbase + C_CONV);
                tg += 4 * LINC;
                lwait(smem + C_CONV, 16);
                asm volatile(TLOAD_ASM :: "v"(tb) : "memory", TLOAD_CLOB);
                STAMP(27);
            }
            fl = ldf(ws, o_fl1, 16384 + L * 8192);
            pollf(ws, o_fl1, 16384 + L * 8192, fl, e1_, dead);
            lwait(smem + C_RY, 4 * L + 4);
        }
        STAMP_OUT();
        return;
    }
    SETPRIO(RPRIO);
    {
        const KG g = kg_prep(kp);
        cf L[9];
        L[1] = CEXP(3); L[2] = CEXP(4); L[4] = CEXP(5); L[8] = CEXP(6);
        L[3] = cmul(L[1], L[2]); L[5] = cmul(L[1], L[4]); L[6] = cmul(L[2], L[4]); L[7] = cmul(L[3], L[4]);
        PUTP(41, 0x3c00u);
        const u32 l8 = pkp(L[8]);
#pragma unroll
        for (int i = 1; i < 8; ++i) { const u32 l = pkp(L[i]); PUTP(41 + i, l); PUTP(49 + i, hmul(l, l8)); }
        PUTP(49, l8);
    }
    lsig(sbase + C_K);
    asm volatile("" : "+v"(kk[0][0]), "+v"(kk[0][1]), "+v"(kk[0][2]), "+v"(kk[0][3]), "+v"(kk[0][4]), "+v"(kk[0][5]), "+v"(kk[0][6]), "+v"(sk4[0]) :: "memory");
    SETPRIO(3);
    STAMP(20);
    u32 qq[2][4], pil, Dsk[4];
    auto q_loads = [&]() {
    {
        const rsrc_t rq = mkr(p.q, SL * DM * 2);
#pragma unroll
        for (int e = 0; e < 2; ++e) {
#pragma unroll
            for (int j = 0; j < 4; ++j) qq[e][j] = ldw(rq, o_rw + j * 1024u + e * 128u, s_rw);
        }
        pil = ldh(rq, lg * 2048, rid * 8192) | (ldh(rq, lg * 2048 + 1024, rid * 8192) << 16);
    }
    {
        const rsrc_t rd2 = mkr(p.Dskip, DM * 2);
#pragma unroll
        for (int k = 0; k < 4; ++k) Dsk[k] = ldw(rd2, li * 4 + k * 64, r * 256);
    }
        FENCE();
    };
    if (Q_POS == 0) q_loads();
    FENCE();
    v4u kvp[2];
    {
#pragma unroll
        for (int e = 0; e < 2; ++e) {
            const float w00 = blo(sk4[e].x), w01 = bhi(sk4[e].x), w02 = blo(sk4[e].y), w03 = bhi(sk4[e].y);
            const float w10 = blo(sk4[e].z), w11 = bhi(sk4[e].z), w12 = blo(sk4[e].w), w13 = bhi(sk4[e].w);
            float a0[4], a1[4];
#pragma unroll
            for (int j = 0; j < 4; ++j) {
                const float s0 = __builtin_fmaf(blo(kk[e][j + 3]), w00, __builtin_fmaf(blo(kk[e][j + 2]), w01, __builtin_fmaf(blo(kk[e][j + 1]), w02, blo(kk[e][j]) * w03)));
                const float s1 = __builtin_fmaf(bhi(kk[e][j + 3]), w10, __builtin_fmaf(bhi(kk[e][j + 2]), w11, __builtin_fmaf(bhi(kk[e][j + 1]), w12, bhi(kk[e][j]) * w13)));
                a0[j] = s0 * blo(vv[e][j]);
                a1[j] = s1 * bhi(vv[e][j]);
            }
            kvp[e] = half_xpose(ns(pkbf(a0[0], a0[1])), ns(pkbf(a0[2], a0[3])), ns(pkbf(a1[0], a1[1])), ns(pkbf(a1[2], a1[3])));
            if (e == 0) lwait(smem + C_K1, 4);
            st16(ws, o_w, s_w + e * 262144u, kvp[e]);
        }
    }
    STAMP(1);
    SETPRIO(XPRIO);
    lsig(sbase + C_ROW);
    if (Q_POS == 1) q_loads();
    int io = (r & 1) == 0 ? 128 * (lg >> 1) + 4 * (lg & 1) + 8 * li : 256 + 64 * lg + (lg < 2 ? 4 : 2) * li;
    int st2 = (r & 1) && lg >= 2 ? 32 : 2;
    pin(io); pin(st2);
    lwait(smem + C_K, 8);
    STAMP(2);
    {
        const char* bb = smem + L_SCR + kch * SCR_CH + (41 + li) * SCR_ROW + lg * 16;
        const char* ab = smem + L_SCR + kch * SCR_CH + ((r & 1) * 16 + li) * SCR_ROW + lg * 16;
        const float ysc = *(const float*)(smem + L_YSC + kch * 4);
        v4f c0 = {0.f, 0.f, 0.f, 0.f}, c1 = c0;
#pragma unroll
        for (int ks = 0; ks < 8; ++ks) {
            const v8h bf = *(const v8h*)(bb + ks * 64);
            c0 = __builtin_amdgcn_mfma_f32_16x16x32_f16(*(const v8h*)(ab + ks * 64), bf, c0, 0, 0, 0);
            c1 = __builtin_amdgcn_mfma_f32_16x16x32_f16(*(const v8h*)(bb - 9 * SCR_ROW + ks * 64), bf, c1, 0, 0, 0);
        }
        SGB(0x100, 10);
#pragma unroll
        for (int ks = 0; ks < 5; ++ks) { SGB(0x8, 2); SGB(0x100, 3); }
        SGB(0x8, 6);
        char* tab = smem + L_KTAB + kch * 1536;
        asm volatile("s_nop 7\n\ts_nop 2" : "+a"(c0), "+a"(c1));
        *(u32*)(tab + io * 2) = pkbf(c0[0] * ysc, c0[1] * ysc);
        *(u32*)(tab + (io + st2) * 2) = pkbf(c0[2] * ysc, c0[3] * ysc);
        const u32 p01 = pkbf(c1[0] * ysc, c1[1] * ysc), p23 = pkbf(c1[2] * ysc, c1[3] * ysc);
        u16* t2 = (u16*)tab + 512 + 64 * lg + li;
        t2[0] = (u16)p01; t2[16] = (u16)(p01 >> 16); t2[32] = (u16)p23; t2[48] = (u16)(p23 >> 16);
    }
    lsig(sbase + C_ROW);
    STAMP(3);
    const u32 o_fl = WS_FLAG + (u32)((cid >> 6) * 256 + (t & 255)) * 8u;
    u32 x[8], yd[8];
    u64 fl;
    Stat s;
    float ga[8], gb[8], ca[8], cb[8];
    {
#pragma unroll
        for (int e = 0; e < 2; ++e) {
            pin(Dch[e]);
            const float d0 = blo(Dch[e]);
            u32 o[4];
            to_rowlay((v4u){pkbf(blo(kvp[e].x) * d0, bhi(kvp[e].x) * d0), pkbf(blo(kvp[e].y) * d0, bhi(kvp[e].y) * d0),
                            pkbf(blo(kvp[e].z) * d0, bhi(kvp[e].z) * d0), pkbf(blo(kvp[e].w) * d0, bhi(kvp[e].w) * d0)}, o);
#pragma unroll
            for (int k = 0; k < 4; ++k) yd[e * 4 + k] = o[k];
        }
#pragma unroll
        for (int k = 0; k < 8; ++k) pin(yd[k]);
#pragma unroll
        for (int e = 0; e < 2; ++e) {
            u32 o[4];
            to_rowlay(half_xpose(__builtin_amdgcn_perm(qq[e][1], qq[e][0], 0x05040100u), __builtin_amdgcn_perm(qq[e][3], qq[e][2], 0x05040100u),
                                 __builtin_amdgcn_perm(qq[e][1], qq[e][0], 0x07060302u), __builtin_amdgcn_perm(qq[e][3], qq[e][2], 0x07060302u)), o);
#pragma unroll
            for (int k = 0; k < 4; ++k) x[e * 4 + k] = o[k];
        }
        s.ma = blo(pil); s.mb = bhi(pil);
        float da[8], db[8], na, nb;
#pragma unroll
        for (int k = 0; k < 8; ++k) { da[k] = blo(x[k]) - s.ma; db[k] = bhi(x[k]) - s.mb; }
        row_stats(smem + L_RR, r, lane, da, db, s, na, nb);
#pragma unroll
        for (int k = 0; k < 8; ++k) {
            const float ea = blo(x[k]) - s.ma, eb = bhi(x[k]) - s.mb;
            const u32 pd = pkbf(ea, eb);
            const u32 n0 = pkbf(blo(pd) * s.ra, bhi(pd) * s.rb);
            ga[k] = blo(n0); gb[k] = bhi(n0);
            ca[k] = __builtin_fmaf(ga[k], blo(yd[k]), ea);
            cb[k] = __builtin_fmaf(gb[k], bhi(yd[k]), eb);
            pin(ca[k]); pin(cb[k]);
        }
    }
    STAMP(7);
    fl = ldf(ws, o_fl, 8192);
    auto taps = [&]() {
    lwait(smem + C_ROW, 8);
    {
        const int c = 255 - (t & 255);
        v4u kc[2];
        if (r < 2) {
#pragma unroll
            for (int e = 0; e < 2; ++e) {
                const char* tab = smem + L_KTAB + e * 1536;
                const u32 a = *(const u16*)(tab + (384 + c) * 2), bq = *(const u16*)(tab + (385 + c) * 2);
                const u32 aa = a | (a << 16), bb = bq | (bq << 16);
                kc[e] = (v4u){aa, aa, (c & 1) ? bb : (a | (bq << 16)), bb};
            }
        } else if (r == 2) {
#pragma unroll
            for (int e = 0; e < 2; ++e) {
                const char* tab = smem + L_KTAB + e * 1536;
                const u32 d0 = *(const u32*)(tab + (256 + 2 * c) * 2), d1 = *(const u32*)(tab + (258 + 2 * c) * 2);
                kc[e] = (v4u){__builtin_amdgcn_perm(d0, d0, 0x01000100u), d0, __builtin_amdgcn_perm(d0, d0, 0x03020302u), __builtin_amdgcn_perm(d1, d1, 0x01000100u)};
            }
        } else {
            const bool s1 = c >= 32;
#pragma unroll
            for (int e = 0; e < 2; ++e) {
                const char* a = smem + L_KTAB + e * 1536 + (s1 ? (128 + 4 * c) * 2 : 16 * c);
                const v2u d01 = *(const v2u*)a, d23 = *(const v2u*)(a + 8);
                kc[e].x = s1 ? __builtin_amdgcn_perm(d01.x, d01.x, 0x01000100u) : d01.x;
                kc[e].y = s1 ? __builtin_amdgcn_alignbit(d01.y, d01.x, 16) : d01.y;
                kc[e].z = s1 ? __builtin_amdgcn_perm(d01.y, d01.y, 0x01000100u) : d23.x;
                kc[e].w = s1 ? __builtin_amdgcn_alignbit(d23.x, d01.y, 16) : d23.y;
            }
        }
        *(v4u*)(smem + L_KL + (c + 1) * 16) = kc[0];
        *(v4u*)(smem + L_KL + KL_CH + (c + 1) * 16) = kc[1];
    }
    lsig(sbase + C_ROW);
    };
    const bool late = lpeek(smem + (half ? C_Y : C_CL), 4 + 4 * half);
    if (!late) taps();
    STAMP(4);
    TAG();
    pollf(ws, o_fl, 8192, fl, e1_, dead);
    STAMP(5);
    SETPRIO(3);
    const u32 o_yr = (u32)lane * 128u;
    const u32 s_yr = WS_Y + (u32)(rid >> 3) * 65536u + (u32)(rid & 7) * 16u + (u32)r * 16384u;
    u32 y[8];
    float da[8], db[8], na, nb;
    if (!lpeek(smem + C_Y, 4 * half + 4)) {
        lwait(smem + C_Y, 4 * half + 4);
        __builtin_amdgcn_s_sleep(DLY_Y);
    }
    STAMP(8);
    get_y(ws, o_yr, s_yr, s_yr + (YL ? 4096u : 8192u), y, dead);
    const u32 c_m1 = sbase + (half ? C_RY : C_DUM), c_m2 = sbase + (half ? C_DUM : C_RY);
    lsig(c_m1);
    lwait(smem + C_CL, 4);
    STAMP(9);
#pragma unroll
    for (int k = 0; k < 8; ++k) {
        da[k] = __builtin_fmaf(ga[k], blo(y[k]), ca[k]);
        db[k] = __builtin_fmaf(gb[k], bhi(y[k]), cb[k]);
    }
    row_stats(smem + L_RR + 256, r, lane, da, db, s, na, nb);
    put_u(ws, o_w, s_w + MB2, da, db, s, na, nb);
    STAMP(11);
    if (late) taps();
    SETPRIO(0);
#pragma unroll
    for (int k = 0; k < 8; ++k) {
        const u32 z = add2(y[k], yd[k]);
        x[k] = add2(x[k], pkbf(ga[k] * blo(z), gb[k] * bhi(z)));
        ga[k] = blo(z); gb[k] = bhi(z);
        const float ea = blo(x[k]) - s.ma, eb = bhi(x[k]) - s.mb;
        const u32 pd = pkbf(ea, eb);
        const u32 u = pkbf(blo(pd) * s.ra, bhi(pd) * s.rb);
        const float dk = DSK(k);
        yd[k] = pkbf(blo(u) * dk, bhi(u) * dk);
        ca[k] = ea + blo(yd[k]);
        cb[k] = eb + bhi(yd[k]);
        pin(ca[k]); pin(cb[k]); pin(ga[k]); pin(gb[k]);
    }
    wait_vm0();
    lsig(c_m2);
    if (b == 0 && t == 256) st8(ws, WS_EPOCH, 0, tag);
    SETPRIO(3);
    STAMP(12);
    if (!lpeek(smem + C_Y, 4 * half + 12)) {
        lwait(smem + C_Y, 4 * half + 12);
        __builtin_amdgcn_s_sleep(DLY_Y);
    }
    STAMP(15);
    get_y(ws, o_yr, s_yr + MB2, s_yr + MB2 + (YL ? 4096u : 8192u), y, dead);
    lsig(c_m1);
    lwait(smem + C_CL, 8);
    STAMP(16);
#pragma unroll
    for (int k = 0; k < 8; ++k) { da[k] = blo(y[k]) + ca[k]; db[k] = bhi(y[k]) + cb[k]; }
    Stat s2 = s;
    row_stats(smem + L_RR + 512, r, lane, da, db, s2, na, nb);
#pragma unroll
    for (int k = 0; k < 8; ++k) {
        da[k] = __builtin_fmaf(__builtin_fmaf(da[k], s2.ra, na), ga[k], da[k]);
        db[k] = __builtin_fmaf(__builtin_fmaf(db[k], s2.rb, nb), gb[k], db[k]);
    }
    STAMP(17);
    row_stats(smem + L_RR + 768, r, lane, da, db, s, na, nb);
    put_u(ws, o_w, s_w + 2 * MB2, da, db, s, na, nb);
    STAMP(18);
    SETPRIO(0);
    {
        char* tr = smem + L_SCR + r * 4352;
#pragma unroll
        for (int k = 0; k < 8; ++k) {
            u32 xx = add2(x[k], add2(y[k], yd[k]));
            const u32 n = ln2(xx, s2);
            xx = add2(xx, pkbf(blo(n) * ga[k], bhi(n) * gb[k]));
            const u32 u = ln2(xx, s);
            const float dk = DSK(k);
            const u32 d = pkbf(blo(u) * dk, bhi(u) * dk);
            const int co = (64 * (k >> 2) + 32 * (k & 1) + ((k >> 1) & 1)) * 4;
            *(float*)(tr + lg * 1088 + li * 8 + co) = blo(xx) + blo(d);
            *(float*)(tr + lg * 1088 + 544 + li * 8 + co) = bhi(xx) + bhi(d);
        }
        wait_vm0();
        lsig(c_m2);
#pragma unroll
        for (int e = 0; e < 2; ++e) {
            const v4f c0 = *(const v4f*)(tr + lg * 1088 + li * 32 + e * 16), c1 = *(const v4f*)(tr + lg * 1088 + 544 + li * 32 + e * 16);
#pragma unroll
            for (int k = 0; k < 4; ++k) { ca[e * 4 + k] = c0[k]; cb[e * 4 + k] = c1[k]; pin(ca[e * 4 + k]); pin(cb[e * 4 + k]); }
        }
    }
    SETPRIO(3);
    STAMP(19);
    if (!lpeek(smem + C_Y, 4 * half + 20)) {
        lwait(smem + C_Y, 4 * half + 20);
        __builtin_amdgcn_s_sleep(DLY_Y);
    }
    STAMP(22);
    get_y(ws, (u32)(li & 8) * 1024u + (u32)(li & 7) * 512u + (u32)(lg >> 1) * 128u + (u32)(lg & 1) * 4096u, s_yr + 2 * MB2, s_yr + 2 * MB2 + 256u, y, dead);
    STAMP(23);
    {
        const rsrc_t ro = mkr(p.out, SL * DM * 2);
        const u32 oo = (u32)lg * 2048u + (u32)li * 16u;
        v4u o0, o1;
#pragma unroll
        for (int k = 0; k < 4; ++k) {
            o0[k] = pkbf(blo(y[2 * k]) + ca[2 * k], blo(y[2 * k + 1]) + ca[2 * k + 1]);
            o1[k] = pkbf(bhi(y[2 * k]) + cb[2 * k], bhi(y[2 * k + 1]) + cb[2 * k + 1]);
        }
        st16o(ro, oo, s_rw, o0);
        st16o(ro, oo + 1024u, s_rw, o1);
    }
    STAMP(28);
}
#if defined(__HIP_DEVICE_COMPILE__)
#pragma clang attribute pop
#endif

extern "C" void kernel_launch(void* const* d_in, const int* in_sizes, int n_in,
                              void* d_out, int out_size, void* d_ws, size_t ws_size,
                              hipStream_t stream) {
    Params p;
    p.q = (const u16*)d_in[0];
    p.k = (const u16*)d_in[1];
    p.v = (const u16*)d_in[2];
    p.sk = (const u16*)d_in[3];
    p.ssmk = (const u16*)d_in[4];
    p.ssmD = (const u16*)d_in[5];
    p.logA = (const u16*)d_in[7];
    p.Aim = (const u16*)d_in[8];
    p.Cr = (const u16*)d_in[9];
    p.Ci = (const u16*)d_in[10];
    p.logdt = (const u16*)d_in[11];
    p.Dskip = (const u16*)d_in[12];
    p.out = (u16*)d_out;
    p.ws = (char*)d_ws;
    mega<<<256, 512, 0, stream>>>(p);
}
```
